```python
import torch

def _softmax(x, axis=-1):
    return torch.softmax(x, dim=axis)
def _gelu(x):
    return torch.nn.functional.gelu(x, approximate="tanh")
def _silu(x):
    return torch.nn.functional.silu(x)
def _sigmoid(x):
    return torch.sigmoid(x)
def _layernorm(x, axis=-1, eps=1e-5):
    m = x.mean(dim=axis, keepdim=True)
    v = x.var(dim=axis, keepdim=True, unbiased=False)
    return (x - m) / torch.sqrt(v + eps)
def _scatter_add(tgt, idx, src, axis=0):
    return tgt.clone().index_add_(axis, idx, src)
def _segment_sum(src, seg, n):
    out = torch.zeros((n,) + src.shape[1:], dtype=src.dtype)
    return out.index_add_(0, seg, src)
def _pad(x, pw):
    flat = tuple(v for pair in reversed(pw) for v in pair)
    return torch.nn.functional.pad(x, flat)

kernel_name = 'concat_heads__ssm_conv_h3__sliding_local_dilated__prompt'

def setup_inputs():

    _w = (lambda a: (lambda t: t.to(torch.bfloat16) if t.is_floating_point() else t)(torch.as_tensor(a)))
    def _A_setup():
        B, S, D = 1, 2048, 576
        ks = 4
        return {
            "q": _w((lambda *s: torch.randn(*s, dtype=torch.bfloat16) * 0.05)(B, S, D)),
            "k": _w((lambda *s: torch.randn(*s, dtype=torch.bfloat16) * 0.8)(B, S, D)),
            "v": _w((lambda *s: torch.randn(*s, dtype=torch.bfloat16) * 0.8)(B, S, D)),
            "shift_k": _w((lambda *s: torch.randn(*s, dtype=torch.bfloat16) * 0.8)(D, ks) * 0.5),
            "ssm_k": _w((lambda *s: torch.randn(*s, dtype=torch.bfloat16) * 1.6)(D, S) * (1.0 / S ** 0.5)),
            "ssm_D": _w((lambda *s: torch.randn(*s, dtype=torch.bfloat16) * 0.8)(D)),
        }
    _A_in = _A_setup()

    def _B_setup():
        B, S, D = 1, 2048, 576
        return {
            "x": _w((lambda *s: torch.randn(*s, dtype=torch.bfloat16) * 0.2)(B, S, D)),
            "wqkv": _w((lambda *s: torch.randn(*s, dtype=torch.bfloat16) * 0.2)(D, 3 * D)),
            "wo": _w((lambda *s: torch.randn(*s, dtype=torch.bfloat16) * 0.05)(D, D)),
        }
    _B_in = _B_setup()

    return {
        "A_q": _A_in["q"],
        "A_k": _A_in["k"],
        "A_v": _A_in["v"],
        "A_shift_k": _A_in["shift_k"],
        "A_ssm_k": _A_in["ssm_k"],
        "A_ssm_D": _A_in["ssm_D"],
        "B_x": _B_in["x"],
        "B_wqkv": _B_in["wqkv"],
        "B_wo": _B_in["wo"],
    }

def reference(A_q, A_k, A_v, A_shift_k, A_ssm_k, A_ssm_D, B_x, B_wqkv, B_wo):
    def _A_ref(q, k, v, shift_k, ssm_k, ssm_D):
        B, S, D = q.shape
        ks = shift_k.shape[-1]
        k_pad = _pad(k, ((0, 0), (ks - 1, 0), (0, 0)))
        k_shift = torch.zeros_like(k)
        for t in range(ks):
            k_shift = k_shift + k_pad[:, t:t + S, :] * shift_k[:, ks - 1 - t][None, None, :]
        kv = k_shift * v
        L = 2 * S
        kv_pad = _pad(kv, ((0, 0), (0, S), (0, 0)))
        kf_pad = _pad(ssm_k, ((0, 0), (0, S)))
        kv_f = (lambda x, axes=(-2,-1): torch.fft.rfft2(x.float(), dim=axes))(kv_pad[:, None, :, :], axes=(1, 2))
        kf_f = (lambda x, axes=(-2,-1): torch.fft.rfft2(x.float(), dim=axes))(kf_pad[:, None, :], axes=(1, 2))
        kf_f = (lambda x, *d: x.permute(*d))(kf_f, 1, 2, 0)
        y_f = kv_f * kf_f[None, :, :, :]
        y = (lambda x, s, axes=(-2,-1): torch.fft.irfft2(x, s=s, dim=axes).to(torch.bfloat16))(y_f, (1, L), axes=(1, 2))[:, 0, :S, :]
        y = y + kv * ssm_D[None, None, :]
        return q * y

    def _B_ref(x, wqkv, wo):
        B, S, D = x.shape
        H = 32; Dh = D // H
        W, dil = 8, 2
        qkv = (lambda x, *d: x.permute(*d))((x @ wqkv).reshape(B, S, 3, H, Dh), 2, 0, 3, 1, 4)
        q, k, v = qkv[0], qkv[1], qkv[2]
        scale = Dh ** -0.5
        pos = torch.arange(S)
        off = torch.arange(W) * dil
        idx = pos[:, None] - off[None, :]
        mask = idx >= 0
        idx_c = torch.where(mask, idx, 0).reshape(-1)
        kg = k[:, :, idx_c, :].reshape(B, H, S, W, Dh)
        vg = v[:, :, idx_c, :].reshape(B, H, S, W, Dh)
        logits = torch.einsum("bhsd,bhswd->bhsw", q, kg) * scale
        logits = torch.where(mask[None, None, :, :], logits, -1e9)
        attn = _softmax(logits, axis=-1)
        o = torch.einsum("bhsw,bhswd->bhsd", attn, vg)
        return (lambda x, *d: x.permute(*d))(o, 0, 2, 1, 3).reshape(B, S, D) @ wo

    a = _A_ref(A_q, A_k, A_v, A_shift_k, A_ssm_k, A_ssm_D)
    b = _B_ref(B_x, B_wqkv, B_wo)
    return torch.concatenate([a, b], axis=-1)
```

```cpp
#include <hip/hip_runtime.h>
#include <hip/hip_bf16.h>
#include <stdint.h>

#define S_ 2048
#define D_ 576
typedef unsigned short u16;
__device__ __forceinline__ float bf2f(u16 x) { return __uint_as_float(((unsigned)x) << 16); }
__device__ __forceinline__ u16 f2bf(float f) { __hip_bfloat16 b = __float2bfloat16(f); return *(u16*)&b; }
__device__ __forceinline__ float rb(float f) { return bf2f(f2bf(f)); }

__global__ void k_kv(const u16* k, const u16* v, const u16* sk, u16* kvT) {
  int i = blockIdx.x * blockDim.x + threadIdx.x;
  int s = i / D_, d = i % D_;
  float acc = 0.f;
  for (int j = 3; j >= 0; --j) {
    float kk = (s - j >= 0) ? bf2f(k[(s - j) * D_ + d]) : 0.f;
    acc = acc + kk * bf2f(sk[d * 4 + j]);
  }
  kvT[d * S_ + s] = f2bf(acc * bf2f(v[i]));
}
__global__ void k_conv(const u16* kvT, const u16* h, const u16* q, const u16* Dv, u16* out) {
  __shared__ float sx[S_], sh[S_];
  int d = blockIdx.x;
  for (int i = threadIdx.x; i < S_; i += blockDim.x) { sx[i] = bf2f(kvT[d * S_ + i]); sh[i] = bf2f(h[d * S_ + i]); }
  __syncthreads();
  for (int s = threadIdx.x; s < S_; s += blockDim.x) {
    float acc = 0.f;
    for (int j = 0; j <= s; ++j) acc += sh[j] * sx[s - j];
    float y = acc + sx[s] * bf2f(Dv[d]);
    out[s * 2 * D_ + d] = f2bf(bf2f(q[s * D_ + d]) * y);
  }
}
__global__ void k_gemm(const u16* A, const u16* B, u16* C, int N, int ldc) {
  int n = blockIdx.x * blockDim.x + threadIdx.x, m = blockIdx.y;
  if (n >= N) return;
  float acc = 0.f;
  for (int k = 0; k < D_; ++k) acc += bf2f(A[m * D_ + k]) * bf2f(B[k * N + n]);
  C[m * ldc + n] = f2bf(acc);
}
__global__ void k_attn(const u16* qkv, u16* o) {
  int i = blockIdx.x * blockDim.x + threadIdx.x;
  int s = i / 32, h = i % 32;
  float l[8], mx = -1e30f;
  const float scale = 0.23570226039551584f;
  for (int w = 0; w < 8; ++w) {
    int t = s - 2 * w;
    float a = 0.f;
    if (t >= 0) { for (int e = 0; e < 18; ++e) a += bf2f(qkv[s * 1728 + h * 18 + e]) * bf2f(qkv[t * 1728 + 576 + h * 18 + e]); a *= scale; }
    else a = -1e9f;
    l[w] = a; mx = fmaxf(mx, a);
  }
  float sum = 0.f;
  for (int w = 0; w < 8; ++w) { l[w] = __expf(l[w] - mx); sum += l[w]; }
  for (int e = 0; e < 18; ++e) {
    float a = 0.f;
    for (int w = 0; w < 8; ++w) { int t = s - 2 * w; if (t < 0) t = 0; a += l[w] / sum * bf2f(qkv[t * 1728 + 1152 + h * 18 + e]); }
    o[s * D_ + h * 18 + e] = f2bf(a);
  }
}

extern "C" void kernel_launch(void* const* d_in, const int* in_sizes, int n_in,
                              void* d_out, int out_size, void* d_ws, size_t ws_size,
                              hipStream_t stream) {
  const u16* q = (const u16*)d_in[0]; const u16* k = (const u16*)d_in[1]; const u16* v = (const u16*)d_in[2];
  const u16* sk = (const u16*)d_in[3]; const u16* h = (const u16*)d_in[4]; const u16* Dv = (const u16*)d_in[5];
  const u16* x = (const u16*)d_in[6]; const u16* wqkv = (const u16*)d_in[7]; const u16* wo = (const u16*)d_in[8];
  u16* out = (u16*)d_out;
  u16* kvT = (u16*)d_ws; u16* qkv = kvT + S_ * D_; u16* o = qkv + S_ * 1728;
  k_kv<<<S_ * D_ / 256, 256, 0, stream>>>(k, v, sk, kvT);
  k_conv<<<D_, 256, 0, stream>>>(kvT, h, q, Dv, out);
  k_gemm<<<dim3(7, S_), 256, 0, stream>>>(x, wqkv, qkv, 1728, 1728);
  k_attn<<<S_ * 32 / 256, 256, 0, stream>>>(qkv, o);
  k_gemm<<<dim3(3, S_), 256, 0, stream>>>(o, wo, out + D_, D_, 2 * D_);
}
```

```cpp
#include <hip/hip_runtime.h>
#include <hip/hip_bf16.h>
#pragma clang diagnostic ignored "-Wint-to-pointer-cast"

typedef unsigned u32;
typedef unsigned short u16;
typedef unsigned long long u64;
typedef __attribute__((ext_vector_type(4))) u32 u32x4;
typedef __attribute__((ext_vector_type(2))) u32 u32x2;
typedef __attribute__((ext_vector_type(4))) float f32x4;
typedef __attribute__((ext_vector_type(2))) float f32x2;
typedef __attribute__((ext_vector_type(8))) __bf16 bf16x8;
typedef __attribute__((ext_vector_type(2))) __bf16 bf16x2;
typedef __attribute__((ext_vector_type(4))) short s16x4;
#define LAS __attribute__((address_space(3)))
#define GAS __attribute__((address_space(1)))
#define DEV __device__ __forceinline__
typedef __amdgpu_buffer_rsrc_t rsrc_t;

constexpr int NT = 768, NWV = 12;
constexpr u32 ACT_BYTES = 2048u * 1152u;
constexpr u32 W_BYTES = 576u * 3456u;
constexpr u32 WO_BYTES = 576u * 1152u;
constexpr u32 OUT_BYTES = 2048u * 2304u;
constexpr u32 WS_EPOCH = 0, WS_CHECK = 4096, WS_KVF = 8192, WS_OF = 16384;
constexpr u32 WS_KVX = 65536, WS_OM = WS_KVX + 2359296, WS_OX = WS_OM + 2097152, WS_END = WS_OX + 262144;
constexpr u32 WS_DIAG = 8u << 20;
constexpr u32 SLOT = 18432;
constexpr u32 L_W = 0, L_X = 5 * SLOT, L_WO = 3 * SLOT, L_EX = 8 * SLOT;
constexpr u32 L_TAG = L_EX + 29 * 512;
constexpr u32 L_SINK = L_TAG + 16;
constexpr u32 LDS_BYTES = L_SINK + 256;
constexpr u32 HSTR = 8384;
constexpr u32 C_H = 0, C_X = 8 * HSTR, C_Q = C_X + 40960, C_P0 = C_X;
static_assert(C_P0 + 8 * 9216 <= L_TAG && C_Q + 32768 <= L_TAG, "lds");
constexpr u64 SPIN_LIMIT = 400000;
#define CODE_BYTES 8192

#define STAMP(n) do {} while (0)

DEV u32 pk(float a, float b) { f32x2 v = {a, b}; return __builtin_bit_cast(u32, __builtin_convertvector(v, bf16x2)); }
DEV float blo(u32 x) { return __uint_as_float(x << 16); }
DEV float bhi(u32 x) { return __uint_as_float(x & 0xffff0000u); }
DEV u32x2 lds_tr(u32 a) { return __builtin_bit_cast(u32x2, __builtin_amdgcn_ds_read_tr16_b64_v4i16((LAS s16x4*)a)); }
DEV u32x4 lds_tr2(u32 a, u32 b) { u32x2 x = lds_tr(a), y = lds_tr(b); return u32x4{x.x, x.y, y.x, y.y}; }
DEV u32x4 lds_r128(u32 a) { return *(const LAS u32x4*)a; }
DEV u32x2 lds_r64(u32 a) { return *(const LAS u32x2*)a; }
DEV u32 lds_r32(u32 a) { return *(const LAS u32*)a; }
DEV u32x4 lds_r4x32(u32 a) { const LAS u32* p = (const LAS u32*)a; return u32x4{p[0], p[1], p[2], p[3]}; }
DEV void lds_w128(u32 a, u32x4 v) { *(LAS u32x4*)a = v; }
DEV void lds_w64(u32 a, u32x2 v) { *(LAS u32x2*)a = v; }
DEV f32x4 mma(u32x4 a, u32x4 b, f32x4 c) { return __builtin_amdgcn_mfma_f32_16x16x32_bf16(__builtin_bit_cast(bf16x8, a), __builtin_bit_cast(bf16x8, b), c, 0, 0, 0); }
DEV f32x4 mma16(u32x2 a, u32x2 b, f32x4 c) { return __builtin_amdgcn_mfma_f32_16x16x16bf16_1k(__builtin_bit_cast(s16x4, a), __builtin_bit_cast(s16x4, b), c, 0, 0, 0); }
DEV float dot2(u32 a, u32 b, float c) { return __builtin_amdgcn_fdot2_f32_bf16(__builtin_bit_cast(bf16x2, a), __builtin_bit_cast(bf16x2, b), c, false); }
DEV u32x2 pk4(f32x4 v) { return u32x2{pk(v[0], v[1]), pk(v[2], v[3])}; }
DEV void swap16(u32& a, u32& b) { auto r = __builtin_amdgcn_permlane16_swap(a, b, false, false); a = r[0]; b = r[1]; }
DEV void swap32(u32& a, u32& b) { auto r = __builtin_amdgcn_permlane32_swap(a, b, false, false); a = r[0]; b = r[1]; }
DEV void swap16f(float& a, float& b) { u32 x = __float_as_uint(a), y = __float_as_uint(b); swap16(x, y); a = __uint_as_float(x); b = __uint_as_float(y); }
DEV void swap32f(float& a, float& b) { u32 x = __float_as_uint(a), y = __float_as_uint(b); swap32(x, y); a = __uint_as_float(x); b = __uint_as_float(y); }
DEV float fmx(float a, float b) { float r; asm("v_max_f32 %0, %1, %2" : "=v"(r) : "v"(a), "v"(b)); return r; }
DEV void bar() { asm volatile("s_waitcnt lgkmcnt(0)\n\ts_barrier" ::: "memory"); }
DEV void bar_raw() { asm volatile("s_barrier" ::: "memory"); }
#define SB() __builtin_amdgcn_sched_barrier(0)
#define WAIT_VM(n) asm volatile("s_waitcnt vmcnt(" #n ")" ::: "memory")

DEV u32x4 mk_srd(const void* p, u32 bytes) { u64 a = (u64)p; return u32x4{(u32)a, (u32)(a >> 32) & 0xffffu, bytes, 0x20000u}; }
DEV rsrc_t mk_rsrc(const void* p, u32 bytes) { return __builtin_amdgcn_make_buffer_rsrc((void*)p, 0, bytes, 0x20000); }
DEV void dma16(u32x4 srd, u32 voff, u32 soff, u32 lds) {
  asm volatile("s_mov_b32 m0, %3\n\ts_nop 0\n\tbuffer_load_dwordx4 %0, %1, %2 offen lds" :: "v"(voff), "s"(srd), "s"(soff), "s"(lds) : "memory");
}
DEV void touch(u32 sink, u32x4 srd, u32 voff, u32 soff) {
  asm volatile("s_mov_b32 m0, %3\n\ts_nop 0\n\tbuffer_load_dword %0, %1, %2 offen lds" :: "v"(voff), "s"(srd), "s"(soff), "s"(sink) : "memory");
}
DEV u64 ld_flag(const char* p) { return __hip_atomic_load((const GAS u64*)p, __ATOMIC_RELAXED, __HIP_MEMORY_SCOPE_AGENT); }
DEV void st_flag(char* p, u64 v) { __hip_atomic_store((GAS u64*)p, v, __ATOMIC_RELAXED, __HIP_MEMORY_SCOPE_AGENT); }
DEV void poll(const char* p, u64 tag) {
  u64 t0 = __builtin_amdgcn_s_memrealtime();
  for (;;) {
    u64 v = ld_flag(p);
    if (__all(v == tag)) break;
    if (__builtin_amdgcn_s_memrealtime() - t0 > SPIN_LIMIT) break;
    __builtin_amdgcn_s_sleep(1);
  }
  asm volatile("" ::: "memory");
}
DEV u64 read_tag(u32 lb) { u32x2 t = lds_r64(lb + L_TAG); return ((u64)t.y << 32) | t.x; }
DEV void epoch_in(char* ws, u32 lb, int lane) {
  u64 tag = ld_flag(ws + WS_EPOCH) + 1;
  if (lane == 0) { lds_w64(lb + L_TAG, u32x2{(u32)tag, (u32)(tag >> 32)}); st_flag(ws + WS_CHECK + blockIdx.x * 8, tag); }
}
DEV void touch_unit(u32 sink, int n, int xcd, u32x4 sx, u32x4 sw, u32 vx, u32 vw) {
  int kc = n / 25, r = n - kc * 25;
  if (r < 9) {
    int row0 = 512 * (xcd >> 1) + (r < 8 ? 64 * r : -64);
    if (row0 >= 0) touch(sink, sx, r < 8 ? vx : (vx < 48 * 1152 ? 0x80000000u : vx), row0 * 1152 + kc * 128);
  } else {
    touch(sink, sw, vw, (kc * 192 + (r - 9) * 12) * 1152 + (xcd & 1) * 512);
  }
}

template <int HSEL>
DEV void gemm1(char* ws, f32x4 (&acc)[2][7], f32x4& hacc, u32 lb, int lane, int pi, int half, int pair, int j) {
  const int g = lane >> 4, q = (lane & 15) >> 2, p = lane & 3, i = lane & 15;
  const u32 off0 = (144 * j) & 63;
  const u32 rowb = lb + L_W + (8 * g + q) * 576;
  const u32 s0 = (g & 1) << 5, s1 = s0 | 16;
  const u32 cA = off0 + 72 * pair + 8 * p, cB = cA + 40, cM = off0 + 72 * pair + 32;
  const u32 wA0 = rowb + (cA ^ s0), wB0 = rowb + (cB ^ s0), wA1 = rowb + 192 + (cA ^ s1), wB1 = rowb + 192 + (cB ^ s1);
  const u32 wM = rowb + (p == 0 ? (cM ^ s0) : p == 1 ? 192 + (cM ^ s1) : 384 + (cM ^ s0));
  const u32 xr = lb + L_X + i * 128 + ((g ^ (i >> 1)) << 4);
  const u32 x0 = xr + (pi * 4 + half * 2) * 2048, xh = xr + 8 * 2048;
  u32x4 w[7], xa[3], xb[3];
  auto rdW = [&](int c, u32 so) -> u32x4 {
    u32 a = c == 0 ? wA0 : c == 1 ? wB0 : c == 2 ? wA1 : c == 3 ? wB1 : c == 4 ? wA0 + 384 : c == 5 ? wB0 + 384 : wM;
    return lds_tr2(a + so, a + so + 4 * 576);
  };
  auto phase = [&](u32x4 (&xc)[3], u32x4 (&xn)[3], u32 xo, u32 xx, u32 wo_) {
    xn[0] = lds_r128((x0 + xo) ^ xx); xn[1] = lds_r128(((x0 + xo) ^ xx) + 2048); xn[2] = lds_r128((xh + xo) ^ xx);
    SB();
#pragma unroll
    for (int c = 0; c < 7; ++c) {
      const bool nat = (c == 4 || c == 5);
      acc[0][c] = nat ? mma(xc[0], w[c], acc[0][c]) : mma(w[c], xc[0], acc[0][c]);
      acc[1][c] = nat ? mma(xc[1], w[c], acc[1][c]) : mma(w[c], xc[1], acc[1][c]);
      if (c == 2 + HSEL) hacc = nat ? mma(xc[2], w[c], hacc) : mma(w[c], xc[2], hacc);
      w[c] = rdW(c, wo_);
      SB();
    }
  };
  bar_raw();
  STAMP(1);
  xa[0] = lds_r128(x0); xa[1] = lds_r128(x0 + 2048); xa[2] = lds_r128(xh);
#pragma unroll
  for (int c = 0; c < 7; ++c) w[c] = rdW(c, 0);
  SB();
  u32 ws_ = 0, xs = 0;
#pragma unroll 1
  for (int k = 0; k < 9; ++k) {
    ws_ = ws_ == 4 * SLOT ? 0 : ws_ + SLOT;
    phase(xa, xb, xs, 64, ws_);
    asm volatile("" :: "v"(xb[0]), "v"(xb[1]), "v"(xb[2]));
    if (k < 8) bar_raw();
    ws_ = ws_ == 4 * SLOT ? 0 : ws_ + SLOT;
    xs = xs == 2 * SLOT ? 0 : xs + SLOT;
    phase(xb, xa, xs, 0, ws_);
  }
}

DEV void load_wo(u32x4 swo, u32 lb, int lane, int cg, int lw) {
  const int r = lane >> 3;
  const u32 vo = r * 1152 + (((lane & 7) ^ (((r >> 1) & 3) << 1)) << 4) + cg * 128 + lw * 9216;
  int src = 72 * (cg & 7);
#pragma unroll 1
  for (int n = 0; n < 72; n += 9) {
#pragma unroll
    for (int m = 0; m < 9; m += 3) dma16(swo, vo, (src + 8 * m) * 1152, lb + L_WO + (n + m + lw) * 1024);
    src = src == 504 ? 0 : src + 72;
  }
}

DEV void gemm2(char* ws, u16* out, u32 lb, int lane, int w, int i2, int cg, u64 tag) {
  const int g = lane >> 4, q = (lane & 15) >> 2, p = lane & 3, u = lane & 15;
  const rsrc_t rws = mk_rsrc(ws, WS_END);
  u32 bw[4], bm[4];
#pragma unroll
  for (int ct = 0; ct < 4; ++ct) {
    bw[ct] = lb + L_WO + (4 * g + q) * 128 + ((ct ^ (((4 * g + q) >> 1) & 3)) << 5) + 8 * p;
    bm[ct] = lb + L_WO + (72 * g + 16 + q) * 128 + ((ct ^ (q >> 1)) << 5) + 8 * p;
  }
  auto rdB = [&](int ks, int ct) -> u32x4 {
    if (ks < 16) { const int e = (ks & 1) << 1; return lds_tr2(bw[ct ^ e] + ks * 4608, bw[ct ^ e ^ 2] + ks * 4608 + 2560); }
    return lds_tr2(bm[ct] + (ks - 16) * 36864, bm[ct ^ 2] + (ks - 16) * 36864 + 4608);
  };
  u32x4 B[2][4];
#pragma unroll
  for (int ct = 0; ct < 4; ++ct) { B[0][ct] = rdB(0, ct); B[1][ct] = rdB(1, ct); }
  poll(ws + WS_OF + ((i2 * 4 + (w >> 1)) * 16 + (lane & 15)) * 8, tag);
  STAMP(6);
  u32x4 A[18];
  const u32 om = WS_OM + (i2 * 8 + w) * 16384;
#pragma unroll
  for (int n = 0; n < 16; ++n) A[n] = __builtin_amdgcn_raw_buffer_load_b128(rws, lane * 16, om + ((n + 2 * cg) & 15) * 1024, 16);
#pragma unroll
  for (int m = 0; m < 2; ++m) {
    const u32 vo = ((4 * m + g + cg) & 7) * 256 + u * 8;
    u32x2 a = __builtin_amdgcn_raw_buffer_load_b64(rws, vo, WS_OX + (i2 * 8 + w) * 2048, 16);
    u32x2 b = __builtin_amdgcn_raw_buffer_load_b64(rws, vo + 128, WS_OX + (i2 * 8 + w) * 2048, 16);
    A[16 + m] = u32x4{a.x, a.y, b.x, b.y};
  }
  SB();
  f32x4 acc[4] = {{0, 0, 0, 0}, {0, 0, 0, 0}, {0, 0, 0, 0}, {0, 0, 0, 0}};
#pragma unroll
  for (int ks = 0; ks < 18; ++ks) {
#pragma unroll
    for (int ct = 0; ct < 4; ++ct) {
      acc[ct] = mma(B[ks & 1][ct], A[ks], acc[ct]);
      if (ks + 2 < 18) B[ks & 1][ct] = rdB(ks + 2, ct);
    }
    SB();
  }
  STAMP(7);
  const u32 st = lb + L_W + w * 2304;
#pragma unroll
  for (int ct = 0; ct < 4; ++ct) lds_w64(st + u * 144 + ct * 32 + g * 8, pk4(acc[ct]));
  const rsrc_t ro = mk_rsrc(out, OUT_BYTES);
#pragma unroll
  for (int n = 0; n < 2; ++n) {
    const int r = 8 * n + (lane >> 3);
    u32x4 v = lds_r128(st + r * 144 + (lane & 7) * 16);
    const int row = 128 * i2 + 32 * (w & 3) + 2 * r + (w >> 2);
    __builtin_amdgcn_raw_buffer_store_b128(v, ro, row * 2304 + (lane & 7) * 16 + 1152 + cg * 128, 0, 0);
  }
}

DEV void role_b(const u16* x, const u16* wq, const u16* wo, char* ws, u32 lb, int xcd, int slot, int w, int lane) {
  const int i = 4 * (xcd >> 1) + (slot >> 2), j = 4 * (xcd & 1) + (slot & 3);
  const int cg = slot >= 9 ? slot - 9 : slot;
  const u32x4 sx = mk_srd(x, ACT_BYTES), sw = mk_srd(wq, W_BYTES);
  const int g = lane >> 4, q = (lane & 15) >> 2, p = lane & 3, u = lane & 15;
  if (w >= 9) {
    const int lw = w - 9;
    u32 vw[3];
#pragma unroll
    for (int n = 0; n < 3; ++n) {
      const int c = 64 * (3 * n + lw) + lane, row = c / 36, rem = c - row * 36, seg = rem / 12, ch = rem - seg * 12;
      vw[n] = row * 3456 + seg * 1152 + ((ch ^ ((((row >> 3) & 1) << 1) | (seg == 1))) << 4);
    }
    const int r8 = lane >> 3;
    const u32 vx0 = r8 * 2304 + (((lane & 7) ^ (r8 >> 1)) << 4), vx1 = vx0 ^ 64;
    u32 xo[3], xd[3];
#pragma unroll
    for (int n = 0; n < 3; ++n) {
      const int t = 3 * n + lw;
      xo[n] = t < 8 ? (128 * i + (t & 3) * 32 + (t >> 2)) * 1152 : (i == 0 ? 0 : (128 * i - 16) * 1152);
      xd[n] = xo[n] + (t < 8 ? 16 * 1152 : 1152);
    }
    const u32 wbase = (144 * j) & ~63;
    auto ldX = [&](int k, u32 xs) {
#pragma unroll
      for (int n = 0; n < 3; ++n) {
        dma16(sx, vx0, xo[n] + k * 128, lb + L_X + xs + (3 * n + lw) * 2048);
        dma16(sx, vx1, xd[n] + k * 128, lb + L_X + xs + (3 * n + lw) * 2048 + 1024);
      }
    };
    auto ldW = [&](int s, u32 wsl) {
#pragma unroll
      for (int hh = 0; hh < 2; ++hh)
#pragma unroll
        for (int n = 0; n < 3; ++n) dma16(sw, vw[n], (s * 32 + hh * 16) * 3456 + wbase, lb + L_W + wsl + (hh * 9 + 3 * n + lw) * 1024);
    };
    ldX(0, 0); ldW(0, 0); ldW(1, SLOT); ldX(1, SLOT);
    u32 wl = 2 * SLOT, xl = 2 * SLOT;
    WAIT_VM(6);
    STAMP(1);
#pragma unroll 1
    for (int k = 0; k < 8; ++k) {
      bar_raw();
      ldW(2 * k + 2, wl); wl = wl == 4 * SLOT ? 0 : wl + SLOT;
      ldW(2 * k + 3, wl); wl = wl == 4 * SLOT ? 0 : wl + SLOT;
      if (k < 7) { ldX(k + 2, xl); xl = xl == 2 * SLOT ? 0 : xl + SLOT; WAIT_VM(6); } else WAIT_VM(0);
    }
    bar_raw();
    STAMP(2);
    load_wo(mk_srd(wo, WO_BYTES), lb, lane, cg, lw);
    STAMP(3);
    bar_raw();
    WAIT_VM(0);
    bar_raw();
    return;
  }
  if (w == 8) {
    epoch_in(ws, lb, lane);
    const u32 off0 = (144 * j) & 63, s0 = (g & 1) << 5;
    const u32 cM = off0 + 72 * (p >> 1) + 32;
    const u32 wM = lb + L_W + (8 * g + q) * 576 + ((p & 1) ? 384 + (cM ^ s0) : 192 + (cM ^ (s0 | 16)));
    const u32 xh = lb + L_X + 16384 + u * 128 + ((g ^ (u >> 1)) << 4);
    f32x4 hacc = {0, 0, 0, 0};
    u32 wr = 0, xr = 0;
#pragma unroll 1
    for (int k = 0; k < 9; ++k) {
      bar_raw();
      u32x4 a0 = lds_r128(xh + xr), a1 = lds_r128((xh + xr) ^ 64);
      u32x4 b0 = lds_tr2(wM + wr, wM + wr + 2304);
      wr = wr == 4 * SLOT ? 0 : wr + SLOT;
      u32x4 b1 = lds_tr2(wM + wr, wM + wr + 2304);
      wr = wr == 4 * SLOT ? 0 : wr + SLOT;
      xr = xr == 2 * SLOT ? 0 : xr + SLOT;
      hacc = mma(b0, a0, hacc); hacc = mma(b1, a1, hacc);
      SB();
    }
    lds_w64(lb + L_EX + 8 * 512 + lane * 8, pk4(hacc));
    bar();
    bar_raw();
    return;
  }
  const int pi = w & 1, half = (w >> 1) & 1, pair = w >> 2;
  if (w < 2) {
    const u32 sink = lb + L_SINK;
    const int n = slot + 16 * w;
    if (n < 25) touch_unit(sink, n, xcd, sx, sw, lane * 1152, lane < 60 ? (lane / 5) * 1152 + (lane % 5) * 128 : 0x80000000u);
  }
  f32x4 acc[2][7], hacc = {0, 0, 0, 0};
#pragma unroll
  for (int c = 0; c < 7; ++c) { acc[0][c] = f32x4{0, 0, 0, 0}; acc[1][c] = f32x4{0, 0, 0, 0}; }
  switch (w & 3) {
    case 0: gemm1<0>(ws, acc, hacc, lb, lane, pi, half, pair, j); break;
    case 1: gemm1<1>(ws, acc, hacc, lb, lane, pi, half, pair, j); break;
    case 2: gemm1<2>(ws, acc, hacc, lb, lane, pi, half, pair, j); break;
    default: gemm1<3>(ws, acc, hacc, lb, lane, pi, half, pair, j); break;
  }
  STAMP(2);
  u32x2 P[2][7];
#pragma unroll
  for (int t = 0; t < 2; ++t)
#pragma unroll
    for (int c = 0; c < 7; ++c) P[t][c] = pk4(acc[t][c]);
  lds_w64(lb + L_EX + w * 512 + lane * 8, pk4(hacc));
  const u32 pkb = lb + L_EX + (9 + (pair * 2 + pi) * 5) * 512;
  if (half == 0) {
#pragma unroll
    for (int c = 2; c < 7; ++c) lds_w64(pkb + (c - 2) * 512 + lane * 8, P[1][c]);
  }
  bar();
  STAMP(9);
  const u64 tag = read_tag(lb);
  u32x2 V[5];
  {
    const u32 hk = lb + L_EX + pair * 2048 + (g * 16 + (u & 7) + 8 * pi) * 8;
    const u32 hv = lb + L_EX + pair * 2048 + 1024 + (((g & 1) + 2 * pi) * 16 + u) * 8;
    const u32 hm = lb + L_EX + 8 * 512 + ((2 * pair + (g >> 1)) * 16 + (u & 7) + 8 * pi) * 8;
    const u32 ak = half ? pkb + lane * 8 : hk, av = half ? pkb + 1024 + lane * 8 : hv, am = half ? pkb + 2048 + lane * 8 : hm;
    V[0] = lds_r64(ak); V[1] = lds_r64(ak + 512); V[2] = lds_r64(av); V[3] = lds_r64(av + 512); V[4] = lds_r64(am);
  }
  const float NEG = -__builtin_inff();
  f32x4 bC, bP, bP0;
  u32 mC[2] = {0, 0}, mP[2] = {0, 0};
  const bool first = (i == 0 && half == 0);
#pragma unroll
  for (int r = 0; r < 4; ++r) {
    const int t = 4 * g + r;
    const bool vc = t <= u && t + 7 >= u, vp = t >= u + 9;
    bC[r] = vc ? 0.f : NEG; bP[r] = vp ? 0.f : NEG; bP0[r] = (vp && !first) ? 0.f : NEG;
    mC[r >> 1] |= vc ? 0xffffu << (16 * (r & 1)) : 0u;
    mP[r >> 1] |= vp ? 0xffffu << (16 * (r & 1)) : 0u;
  }
  const u32x2 sel = {g == 3 ? (u == 0 ? 0x3f80u : u == 1 ? 0x3f800000u : 0u) : 0u, g == 3 ? (u == 2 ? 0x3f80u : u == 3 ? 0x3f800000u : 0u) : 0u};
  const f32x4 Z = {0, 0, 0, 0};
  u32x2 VX[3] = {pk4(mma16(V[4], sel, Z)), pk4(mma16(P[0][6], sel, Z)), pk4(mma16(P[1][6], sel, Z))};
  float M[4][4];
#pragma unroll
  for (int t = 0; t < 2; ++t)
#pragma unroll
    for (int hd = 0; hd < 2; ++hd) {
      u32 a = 0, m = P[t][6][hd];
      swap16(a, m);
      a = g == 1 ? a : 0u;
      const u32x4 Q = {P[t][hd].x, P[t][hd].y, a, 0u};
      const u32x4 Kc = {P[t][2 + hd].x, P[t][2 + hd].y, P[t][6][hd], 0u};
      const u32x2 kp = t ? P[0][2 + hd] : V[hd];
      const u32x4 Kp = {kp.x, kp.y, t ? P[0][6][hd] : V[4][hd], 0u};
      const f32x4 sc = mma(Kc, Q, bC), sp = mma(Kp, Q, t ? bP : bP0);
#pragma unroll
      for (int r = 0; r < 4; ++r) M[t * 2 + hd][r] = __builtin_fmaxf(sc[r], sp[r]);
    }
  float X[4], Y[4];
#pragma unroll
  for (int r = 0; r < 4; ++r) {
    swap32f(M[0][r], M[1][r]); swap32f(M[2][r], M[3][r]);
    X[r] = __builtin_fmaxf(M[0][r], M[1][r]); Y[r] = __builtin_fmaxf(M[2][r], M[3][r]);
    swap16f(X[r], Y[r]);
  }
  const float cs = 0.23570226039551584f * 1.4426950408889634f;
  const float mx = __builtin_fmaxf(__builtin_fmaxf(__builtin_fmaxf(X[0], X[1]), __builtin_fmaxf(X[2], X[3])), __builtin_fmaxf(__builtin_fmaxf(Y[0], Y[1]), __builtin_fmaxf(Y[2], Y[3])));
  const float nm = -mx * cs;
  float sum = 0.f;
#pragma unroll
  for (int r = 0; r < 4; ++r) { X[r] = __builtin_amdgcn_exp2f(__builtin_fmaf(X[r], cs, nm)); Y[r] = __builtin_amdgcn_exp2f(__builtin_fmaf(Y[r], cs, nm)); sum += X[r] + Y[r]; }
  const float rs = __builtin_amdgcn_rcpf(sum);
  u32 G[4][2];
#pragma unroll
  for (int e = 0; e < 2; ++e) {
    u32 a = pk(X[2 * e] * rs, X[2 * e + 1] * rs), b = pk(Y[2 * e] * rs, Y[2 * e + 1] * rs);
    swap16(a, b);
    u32 a2 = a, b2 = b;
    swap32(a, a2); swap32(b, b2);
    G[0][e] = a; G[1][e] = a2; G[2][e] = b; G[3][e] = b2;
  }
  const rsrc_t rws = mk_rsrc(ws, WS_END);
#pragma unroll
  for (int t = 0; t < 2; ++t) {
    f32x4 o[2], ox[2];
#pragma unroll
    for (int hd = 0; hd < 2; ++hd) {
      const u32* gg = G[t * 2 + hd];
      const u32x4 Pm = {gg[0] & mP[0], gg[1] & mP[1], gg[0] & mC[0], gg[1] & mC[1]};
      const u32x2 vp = t ? P[0][4 + hd] : V[2 + hd];
      o[hd] = mma(u32x4{vp.x, vp.y, P[t][4 + hd].x, P[t][4 + hd].y}, Pm, Z);
      ox[hd] = mma(u32x4{VX[t].x, VX[t].y, VX[t + 1].x, VX[t + 1].y}, Pm, Z);
    }
    const int rt = pi * 4 + half * 2 + t;
    const u32x2 oa = pk4(o[0]), ob = pk4(o[1]);
    __builtin_amdgcn_raw_buffer_store_b128(u32x4{oa.x, oa.y, ob.x, ob.y}, rws, lane * 16 + WS_OM + ((i * 8 + rt) * 16 + 2 * j + pair) * 1024, 0, 16);
    if (g == 0) __builtin_amdgcn_raw_buffer_store_b64(u32x2{pk(ox[0][0], ox[0][1]), pk(ox[1][2], ox[1][3])}, rws, u * 8 + WS_OX + ((i * 8 + rt) * 16 + 2 * j + pair) * 128, 0, 16);
  }
  STAMP(3);
  WAIT_VM(0);
  if (lane == 0) st_flag(ws + WS_OF + ((i * 4 + pi * 2 + half) * 16 + 2 * j + pair) * 8, tag);
  STAMP(4);
  bar_raw();
  STAMP(5);
}

DEV void role_p0(const u16* k, const u16* v, const u16* sk, char* ws, u32 lb, int a, int blk, int w, int lane) {
  const u32x4 sk_ = mk_srd(k, ACT_BYTES), sv = mk_srd(v, ACT_BYTES);
  const int s0 = 256 * blk + 32 * w, r8 = lane >> 3;
  const u32 st = lb + C_P0 + w * 9216;
  const u32 vo = r8 * 1152 + (((lane & 7) ^ (((r8 >> 1) & 1) << 2)) << 4);
  const u32 so = s0 * 1152 + a * 128;
  if (lane >= 32) dma16(sk_, vo, s0 ? so - 8 * 1152 : so, st);
#pragma unroll
  for (int n = 0; n < 4; ++n) { dma16(sk_, vo, so + n * 9216, st + 1024 + n * 1024); dma16(sv, vo, so + n * 9216, st + 5120 + n * 1024); }
  const u32x2 wv = *(const u32x2*)(sk + (a * 64 + lane) * 4);
  const u32 w10 = __builtin_amdgcn_alignbit(wv.x, wv.x, 16), w32 = __builtin_amdgcn_alignbit(wv.y, wv.y, 16);
  WAIT_VM(0);
  STAMP(1);
  const int g = lane >> 4, q = (lane & 15) >> 2, p = lane & 3;
  const u32 ra = st + q * 128 + ((32 * g + 8 * p) ^ ((q >> 1) << 6));
  u32 Kd[18], Vd[16];
#pragma unroll
  for (int n = 0; n < 9; ++n) { u32x2 t = lds_tr(ra + 512 + n * 512); Kd[2 * n] = t.x; Kd[2 * n + 1] = t.y; }
#pragma unroll
  for (int n = 0; n < 8; ++n) { u32x2 t = lds_tr(ra + 5120 + n * 512); Vd[2 * n] = t.x; Vd[2 * n + 1] = t.y; }
  if (s0 == 0) { Kd[0] = 0; Kd[1] = 0; }
  const rsrc_t rws = mk_rsrc(ws, WS_END);
  u32 ko = __builtin_amdgcn_alignbit(Kd[1], Kd[0], 16);
#pragma unroll
  for (int n = 0; n < 4; ++n) {
    u32x4 o;
#pragma unroll
    for (int e = 0; e < 4; ++e) {
      const int m = 4 * n + e;
      const u32 kn = __builtin_amdgcn_alignbit(Kd[m + 2], Kd[m + 1], 16);
      const float ev = dot2(kn, w10, dot2(ko, w32, 0.f)) * blo(Vd[m]);
      const float od = dot2(Kd[m + 2], w10, dot2(Kd[m + 1], w32, 0.f)) * bhi(Vd[m]);
      ko = kn;
      o[3 - e] = pk(od, ev);
    }
    const int oc = 32 * blk + 4 * w + n;
    __builtin_amdgcn_raw_buffer_store_b128(o, rws, r8 * 32768 + (((lane & 7) ^ ((oc >> 1) & 7)) << 4) + WS_KVX + a * 262144 + oc * 128, 0, 16);
  }
}

template <bool EXTRA>
DEV void role_conv(const u16* qp, const u16* h, const u16* Dv, u16* out, char* ws, u32 lb, int a, int sub, int w, int lane, bool didp0, int pa, int pblk) {
  const int g = lane >> 4, r = lane & 15;
  const int ch0 = a * 64 + sub * 8;
  if (w >= 8) {
    bar();
    bar_raw();
    const u32x4 sq = mk_srd(qp, ACT_BYTES);
    const int rot = EXTRA ? 2 * sub : 2 * sub;
#pragma unroll 1
    for (int n = w - 8; n < 16; n += 4) {
      const int bl = (n + rot) & 15;
      if (EXTRA) {
        dma16(sq, (lane >> 1) * 1152 + (lane & 1) * 16, (bl * 64) * 1152 + ch0 * 2, lb + C_Q + bl * 2048);
        dma16(sq, (lane >> 1) * 1152 + (lane & 1) * 16, (bl * 64 + 32) * 1152 + ch0 * 2, lb + C_Q + bl * 2048 + 1024);
      } else {
        dma16(sq, lane * 1152, (1024 + bl * 64) * 1152 + ch0 * 2, lb + C_Q + bl * 1024);
      }
    }
    WAIT_VM(0);
    bar_raw();
    return;
  }
  STAMP(9);
  u32x4 H[4];
#pragma unroll
  for (int n = 0; n < 4; ++n) {
    const int ch = EXTRA ? ch0 + 8 * (n >> 1) + w : ch0 + w, e0 = EXTRA ? (n & 1) * 512 : n * 512;
    H[n] = *(const u32x4*)(h + ch * 2048 + e0 + lane * 8);
  }
  u32 dv[2];
  dv[0] = Dv[ch0 + w]; dv[1] = EXTRA ? Dv[ch0 + 8 + w] : 0;
  bar_raw();
  const u64 tag = read_tag(lb);
  if (didp0) {
    WAIT_VM(0);
    if (lane == 0) st_flag(ws + WS_KVF + ((pa * 8 + pblk) * 8 + w) * 8, tag);
  }
  STAMP(2);
  constexpr u32 OOFF = EXTRA ? 2112 : 4160;
  const u32 hb = lb + C_H + w * HSTR;
  if (lane < (EXTRA ? 8 : 4)) lds_w128(hb + (lane >> 2) * 4192 + ((lane >> 1) & 1) * OOFF + (lane & 1) * 16, u32x4{0, 0, 0, 0});
  lds_w64(lb + C_X + (w * 64 + lane) * 8, u32x2{0, 0});
  if (EXTRA) lds_w64(lb + C_X + 20480 + (w * 64 + lane) * 8, u32x2{0, 0});
  u32 pv[4];
#pragma unroll
  for (int n = 0; n < 4; ++n) lds_w128(hb + (EXTRA ? (n >> 1) * 4192 + (n & 1) * 1024 : n * 1024) + 32 + lane * 16, H[n]);
#pragma unroll
  for (int n = 0; n < 4; ++n) pv[n] = lds_r32(hb + (EXTRA ? (n >> 1) * 4192 + (n & 1) * 1024 : n * 1024) + 28 + lane * 16);
#pragma unroll
  for (int n = 0; n < 4; ++n) {
    const u32x4 o = {__builtin_amdgcn_alignbit(H[n].x, pv[n], 16), __builtin_amdgcn_alignbit(H[n].y, H[n].x, 16), __builtin_amdgcn_alignbit(H[n].z, H[n].y, 16), __builtin_amdgcn_alignbit(H[n].w, H[n].z, 16)};
    lds_w128(hb + OOFF + (EXTRA ? (n >> 1) * 4192 + (n & 1) * 1024 : n * 1024) + 32 + lane * 16, o);
  }
  const int blk = EXTRA ? (w & 3) : w, set = EXTRA ? (w >> 2) : 0;
  poll(ws + WS_KVF + ((a * 8 + blk) * 8 + (lane & 7)) * 8, tag);
  STAMP(3);
  const rsrc_t rws = mk_rsrc(ws, WS_END);
  u32x4 xv[4];
#pragma unroll
  for (int n = 0; n < 4; ++n) xv[n] = __builtin_amdgcn_raw_buffer_load_b128(rws, lane * 16 + n * 1024, WS_KVX + (a * 8 + sub + set) * 32768 + blk * 4096, 16);
#pragma unroll
  for (int n = 0; n < 4; ++n) lds_w128(lb + C_X + set * 20480 + 4096 + blk * 4096 + n * 1024 + lane * 16, xv[n]);
  bar();
  STAMP(4);
  const u32 aa = hb + ((r & 1) ? 0 : OOFF) + ((r >> 1) + 1 + 4 * g) * 4;
  auto baddr = [&](int b) -> u32 { const int o = 33 + 2 * r - g - 4 * b; return lb + C_X + o * 128 + ((w ^ ((o >> 1) & 7)) << 4); };
  auto boff = [](int c) -> u32 { return EXTRA ? (c >> 2) * 20480 + (c & 3) * 4096 : c * 4096; };
  auto aoff = [](int c) -> u32 { return EXTRA ? (c >> 2) * 4192 + (c & 3) * 512 : c * 512; };
  constexpr int NA = EXTRA ? 8 : 4;
  f32x4 acc[NA];
  {
    const int jd = 15 - r - 8 * g;
    const u32 b0 = baddr(0);
#pragma unroll
    for (int t = 0; t < NA; ++t) {
      const u32 d = dv[EXTRA ? t >> 2 : 0] << (16 * (jd & 1));
      const u32x4 AD = {(jd >> 1) == 0 ? d : 0u, (jd >> 1) == 1 ? d : 0u, (jd >> 1) == 2 ? d : 0u, (jd >> 1) == 3 ? d : 0u};
      acc[t] = mma(AD, lds_r128(b0 + boff(EXTRA ? t : 4 + t)), f32x4{0, 0, 0, 0});
    }
  }
  u32x4 A[8], B[8];
  {
    const u32 ba = baddr(0);
#pragma unroll
    for (int c = 0; c < 8; ++c) { const int cb = EXTRA ? c ^ 3 : 7 - c; A[c] = lds_r4x32(aa + aoff(c)); B[cb] = lds_r128(ba + boff(cb)); }
  }
  SB();
#pragma unroll 1
  for (int b = 1; b < 9; ++b) {
    const u32 ba = baddr(b & 7), ab = aa + 64 * (b & 7);
#pragma unroll
    for (int c = 0; c < 8; ++c) {
      if (EXTRA) {
#pragma unroll
        for (int t = (c | 3); t >= c; --t) acc[t] = mma(A[c], B[(c & 4) + t - c], acc[t]);
      } else {
#pragma unroll
        for (int t = 7; t >= (c > 4 ? c : 4); --t) acc[t - 4] = mma(A[c], B[t - c], acc[t - 4]);
      }
      SB();
      const int cb = EXTRA ? c ^ 3 : 7 - c;
      A[c] = lds_r4x32(ab + aoff(c)); B[cb] = lds_r128(ba + boff(cb));
      SB();
    }
  }
  STAMP(5);
#pragma unroll
  for (int t = 0; t < NA; ++t) lds_w64(hb + t * 512 + r * 32 + g * 8, pk4(acc[t]));
  bar();
  STAMP(6);
  const rsrc_t ro = mk_rsrc(out, OUT_BYTES);
  const int q = r >> 2, p = lane & 3;
#pragma unroll
  for (int n = 0; n < 2; ++n)
#pragma unroll
    for (int s = 0; s < (EXTRA ? 2 : 1); ++s) {
      const int row = w * 128 + n * 64;
      const u32 za = lb + C_H + q * HSTR + s * 2048 + (row + 16 * g) * 2 + 8 * p;
      const u32x4 z = lds_tr2(za, za + 4 * HSTR);
      const u32x4 qq = lds_r128(lb + C_Q + (EXTRA ? (row + lane) * 32 + s * 16 : (row + lane) * 16));
      u32x4 o;
#pragma unroll
      for (int e = 0; e < 4; ++e) o[e] = pk(blo(z[e]) * blo(qq[e]), bhi(z[e]) * bhi(qq[e]));
      __builtin_amdgcn_raw_buffer_store_b128(o, ro, lane * 2304 + ((EXTRA ? 0 : 1024) + row) * 2304 + ch0 * 2 + s * 16, 0, 0);
    }
}

__global__ __launch_bounds__(NT) void fused_kernel(const u16* __restrict__ q, const u16* __restrict__ k, const u16* __restrict__ v,
                                                   const u16* __restrict__ sk, const u16* __restrict__ h, const u16* __restrict__ Dv,
                                                   const u16* __restrict__ x, const u16* __restrict__ wq, const u16* __restrict__ wo,
                                                   u16* __restrict__ out, char* __restrict__ ws) {
  __shared__ __attribute__((aligned(16))) char lds[LDS_BYTES];
  const u32 lb = (u32)(size_t)(LAS char*)lds;
  const int xcd = blockIdx.x & 7, slot = blockIdx.x >> 3;
  const int w = __builtin_amdgcn_readfirstlane(threadIdx.x >> 6), lane = threadIdx.x & 63;
  STAMP(0);
  if (slot < 16) {
    role_b(x, wq, wo, ws, lb, xcd, slot, w, lane);
  } else {
  const bool p0 = slot <= 24;
  if (w >= 8) {
    if (w == 8) epoch_in(ws, lb, lane);
  } else if (p0) {
    role_p0(k, v, sk, ws, lb, slot - 16, xcd, w, lane);
  } else if (w == 0) {
    const u32 sink = lb + L_SINK;
    const u32x4 sx = mk_srd(x, ACT_BYTES), sw = mk_srd(wq, W_BYTES), so = mk_srd(wo, WO_BYTES);
    const u32 vx = lane * 1152, vw = lane < 60 ? (lane / 5) * 1152 + (lane % 5) * 128 : 0x80000000u;
#pragma unroll 1
    for (int n = 25 + slot - 25; n < 225; n += 7) touch_unit(sink, n, xcd, sx, sw, vx, vw);
#pragma unroll 1
    for (int n = slot - 25; n < 81; n += 7) touch(sink, so, lane * 128, n * 8192);
    WAIT_VM(0);
    STAMP(1);
  }
  const bool ninth = slot == 24 || slot == 25;
  const int ca = ninth ? 8 : xcd;
  if (slot < 24 || (ninth && xcd < 4)) role_conv<false>(q, h, Dv, out, ws, lb, ca, ninth ? 2 * xcd + slot - 24 : slot - 16, w, lane, p0, slot - 16, xcd);
  else if (slot < 30 && (!ninth || xcd < 6)) role_conv<true>(q, h, Dv, out, ws, lb, ca, ninth ? 4 * (xcd - 4) + 2 * (slot - 24) : 2 * (slot - 26), w, lane, p0, slot - 16, xcd);
  else {
    if (slot >= 30 && w >= 9) load_wo(mk_srd(wo, WO_BYTES), lb, lane, slot - 23, w - 9);
    WAIT_VM(0);
    bar();
    const u64 tag = read_tag(lb);
    if (p0) {
      if (w < 8 && lane == 0) st_flag(ws + WS_KVF + ((8 * 8 + xcd) * 8 + w) * 8, tag);
    } else if (slot == 25 && xcd == 7 && w == 0) {
#pragma unroll 1
      for (int n = 0; n < 4; ++n) poll(ws + WS_CHECK + (n * 64 + lane) * 8, tag);
      if (lane == 0) st_flag(ws + WS_EPOCH, tag);
    }
  }
  }
  if ((slot < 16 || slot >= 30) && w < 8) gemm2(ws, out, lb, lane, w, 2 * xcd + (slot >= 9), slot < 9 ? slot : slot < 16 ? slot - 9 : slot - 23, read_tag(lb));
  STAMP(8);
}

extern "C" void kernel_launch(void* const* d_in, const int* in_sizes, int n_in, void* d_out, int out_size, void* d_ws, size_t ws_size,
                              hipStream_t stream) {
  fused_kernel<<<256, NT, 0, stream>>>((const u16*)d_in[0], (const u16*)d_in[1], (const u16*)d_in[2], (const u16*)d_in[3], (const u16*)d_in[4],
                                       (const u16*)d_in[5], (const u16*)d_in[6], (const u16*)d_in[7], (const u16*)d_in[8], (u16*)d_out, (char*)d_ws);
}
```

```cpp
#define AUXA 0
#define AUXB 0
#include <hip/hip_runtime.h>
#include <hip/hip_bf16.h>
#pragma clang diagnostic ignored "-Wint-to-pointer-cast"

typedef unsigned u32;
typedef unsigned short u16;
typedef unsigned long long u64;
typedef __attribute__((ext_vector_type(4))) u32 u32x4;
typedef __attribute__((ext_vector_type(2))) u32 u32x2;
typedef __attribute__((ext_vector_type(4))) float f32x4;
typedef __attribute__((ext_vector_type(2))) float f32x2;
typedef __attribute__((ext_vector_type(8))) __bf16 bf16x8;
typedef __attribute__((ext_vector_type(2))) __bf16 bf16x2;
typedef __attribute__((ext_vector_type(4))) short s16x4;
#define LAS __attribute__((address_space(3)))
#define GAS __attribute__((address_space(1)))
#define DEV __device__ __forceinline__
typedef __amdgpu_buffer_rsrc_t rsrc_t;

constexpr int NT = 768, NWV = 12;
constexpr u32 ACT_BYTES = 2048u * 1152u;
constexpr u32 W_BYTES = 576u * 3456u;
constexpr u32 WO_BYTES = 576u * 1152u;
constexpr u32 OUT_BYTES = 2048u * 2304u;
constexpr u32 WS_EPOCH = 0, WS_CHECK = 4096, WS_KVF = 8192, WS_OF = 16384;
constexpr u32 WS_KVX = 65536, WS_OM = WS_KVX + 2359296, WS_OX = WS_OM + 2097152, WS_END = WS_OX + 262144;
constexpr u32 WS_DIAG = 8u << 20;
constexpr u32 SLOT = 18432;
constexpr u32 L_W = 0, L_X = 5 * SLOT, L_WO = 3 * SLOT, L_EX = 8 * SLOT;
constexpr u32 L_TAG = L_EX + 29 * 512;
constexpr u32 L_SINK = L_TAG + 16;
constexpr u32 LDS_BYTES = L_SINK + 256;
constexpr u32 HSTR = 8384;
constexpr u32 C_H = 0, C_X = 8 * HSTR, C_Q = C_X + 40960, C_P0 = C_X;
static_assert(C_P0 + 8 * 9216 <= L_TAG && C_Q + 32768 <= L_TAG, "lds");
constexpr u64 SPIN_LIMIT = 400000;
#define CODE_BYTES 8192

#define STAMP(n) do {} while (0)
#define DG
#define DA

DEV u32 pk(float a, float b) { f32x2 v = {a, b}; return __builtin_bit_cast(u32, __builtin_convertvector(v, bf16x2)); }
DEV float blo(u32 x) { return __uint_as_float(x << 16); }
DEV float bhi(u32 x) { return __uint_as_float(x & 0xffff0000u); }
DEV u32x2 lds_tr(u32 a) { return __builtin_bit_cast(u32x2, __builtin_amdgcn_ds_read_tr16_b64_v4i16((LAS s16x4*)a)); }
DEV u32x4 lds_tr2(u32 a, u32 b) { u32x2 x = lds_tr(a), y = lds_tr(b); return u32x4{x.x, x.y, y.x, y.y}; }
DEV u32x4 lds_r128(u32 a) { return *(const LAS u32x4*)a; }
DEV u32x2 lds_r64(u32 a) { return *(const LAS u32x2*)a; }
DEV u32 lds_r32(u32 a) { return *(const LAS u32*)a; }
DEV u32x4 lds_r4x32(u32 a) { const LAS u32* p = (const LAS u32*)a; return u32x4{p[0], p[1], p[2], p[3]}; }
DEV void lds_w128(u32 a, u32x4 v) { *(LAS u32x4*)a = v; }
DEV void lds_w64(u32 a, u32x2 v) { *(LAS u32x2*)a = v; }
DEV f32x4 mma(u32x4 a, u32x4 b, f32x4 c) { return __builtin_amdgcn_mfma_f32_16x16x32_bf16(__builtin_bit_cast(bf16x8, a), __builtin_bit_cast(bf16x8, b), c, 0, 0, 0); }
DEV f32x4 mma16(u32x2 a, u32x2 b, f32x4 c) { return __builtin_amdgcn_mfma_f32_16x16x16bf16_1k(__builtin_bit_cast(s16x4, a), __builtin_bit_cast(s16x4, b), c, 0, 0, 0); }
DEV float dot2(u32 a, u32 b, float c) { return __builtin_amdgcn_fdot2_f32_bf16(__builtin_bit_cast(bf16x2, a), __builtin_bit_cast(bf16x2, b), c, false); }
DEV u32x2 pk4(f32x4 v) { return u32x2{pk(v[0], v[1]), pk(v[2], v[3])}; }
DEV void swap16(u32& a, u32& b) { auto r = __builtin_amdgcn_permlane16_swap(a, b, false, false); a = r[0]; b = r[1]; }
DEV void swap32(u32& a, u32& b) { auto r = __builtin_amdgcn_permlane32_swap(a, b, false, false); a = r[0]; b = r[1]; }
DEV void swap16f(float& a, float& b) { u32 x = __float_as_uint(a), y = __float_as_uint(b); swap16(x, y); a = __uint_as_float(x); b = __uint_as_float(y); }
DEV void swap32f(float& a, float& b) { u32 x = __float_as_uint(a), y = __float_as_uint(b); swap32(x, y); a = __uint_as_float(x); b = __uint_as_float(y); }
DEV float fmx(float a, float b) { float r; asm("v_max_f32 %0, %1, %2" : "=v"(r) : "v"(a), "v"(b)); return r; }
DEV void bar() { asm volatile("s_waitcnt lgkmcnt(0)\n\ts_barrier" ::: "memory"); }
DEV void bar_raw() { asm volatile("s_barrier" ::: "memory"); }
#define SB() __builtin_amdgcn_sched_barrier(0)
#define WAIT_VM(n) asm volatile("s_waitcnt vmcnt(" #n ")" ::: "memory")

DEV u32x4 mk_srd(const void* p, u32 bytes) { u64 a = (u64)p; return u32x4{(u32)a, (u32)(a >> 32) & 0xffffu, bytes, 0x20000u}; }
DEV rsrc_t mk_rsrc(const void* p, u32 bytes) { return __builtin_amdgcn_make_buffer_rsrc((void*)p, 0, bytes, 0x20000); }
DEV void dma16(u32x4 srd, u32 voff, u32 soff, u32 lds) {
  asm volatile("s_mov_b32 m0, %3\n\ts_nop 0\n\tbuffer_load_dwordx4 %0, %1, %2 offen lds" :: "v"(voff), "s"(srd), "s"(soff), "s"(lds) : "memory");
}
DEV void touch(u32 sink, u32x4 srd, u32 voff, u32 soff) {
  asm volatile("s_mov_b32 m0, %3\n\ts_nop 0\n\tbuffer_load_dword %0, %1, %2 offen lds" :: "v"(voff), "s"(srd), "s"(soff), "s"(sink) : "memory");
}
DEV u64 ld_flag(const char* p) { return __hip_atomic_load((const GAS u64*)p, __ATOMIC_RELAXED, __HIP_MEMORY_SCOPE_AGENT); }
DEV void st_flag(char* p, u64 v) { __hip_atomic_store((GAS u64*)p, v, __ATOMIC_RELAXED, __HIP_MEMORY_SCOPE_AGENT); }
DEV void poll(const char* p, u64 tag) {
  u64 t0 = __builtin_amdgcn_s_memrealtime();
  for (;;) {
    u64 v = ld_flag(p);
    if (__all(v == tag)) break;
    if (__builtin_amdgcn_s_memrealtime() - t0 > SPIN_LIMIT) break;
    __builtin_amdgcn_s_sleep(1);
  }
  asm volatile("" ::: "memory");
}
DEV u64 read_tag(u32 lb) { u32x2 t = lds_r64(lb + L_TAG); return ((u64)t.y << 32) | t.x; }
DEV void epoch_in(char* ws, u32 lb, int lane) {
  u64 tag = ld_flag(ws + WS_EPOCH) + 1;
  if (lane == 0) { lds_w64(lb + L_TAG, u32x2{(u32)tag, (u32)(tag >> 32)}); st_flag(ws + WS_CHECK + blockIdx.x * 8, tag); }
}
DEV void touch_unit(u32 sink, int n, int xcd, u32x4 sx, u32x4 sw, u32 vx, u32 vw) {
  int kc = n / 25, r = n - kc * 25;
  if (r < 9) {
    int row0 = 512 * (xcd >> 1) + (r < 8 ? 64 * r : -64);
    if (row0 >= 0) touch(sink, sx, r < 8 ? vx : (vx < 48 * 1152 ? 0x80000000u : vx), row0 * 1152 + kc * 128);
  } else {
    touch(sink, sw, vw, (kc * 192 + (r - 9) * 12) * 1152 + (xcd & 1) * 512);
  }
}

template <int HSEL>
DEV void gemm1(DG f32x4 (&acc)[2][7], f32x4& hacc, u32 lb, int lane, int pi, int half, int pair, int j) {
  const int g = lane >> 4, q = (lane & 15) >> 2, p = lane & 3, i = lane & 15;
  const u32 off0 = (144 * j) & 63;
  const u32 rowb = lb + L_W + (8 * g + q) * 576;
  const u32 s0 = (g & 1) << 5, s1 = s0 | 16;
  const u32 cA = off0 + 72 * pair + 8 * p, cB = cA + 40, cM = off0 + 72 * pair + 32;
  const u32 wA0 = rowb + (cA ^ s0), wB0 = rowb + (cB ^ s0), wA1 = rowb + 192 + (cA ^ s1), wB1 = rowb + 192 + (cB ^ s1);
  const u32 wM = rowb + (p == 0 ? (cM ^ s0) : p == 1 ? 192 + (cM ^ s1) : 384 + (cM ^ s0));
  const u32 xr = lb + L_X + i * 128 + ((g ^ (i >> 1)) << 4);
  const u32 x0 = xr + (pi * 4 + half * 2) * 2048, xh = xr + 8 * 2048;
  u32x4 w[7], xa[3], xb[3];
  auto rdW = [&](int c, u32 so) -> u32x4 {
    u32 a = c == 0 ? wA0 : c == 1 ? wB0 : c == 2 ? wA1 : c == 3 ? wB1 : c == 4 ? wA0 + 384 : c == 5 ? wB0 + 384 : wM;
    return lds_tr2(a + so, a + so + 4 * 576);
  };
  auto phase = [&](u32x4 (&xc)[3], u32x4 (&xn)[3], u32 xo, u32 xx, u32 wo_) {
    xn[0] = lds_r128((x0 + xo) ^ xx); xn[1] = lds_r128(((x0 + xo) ^ xx) + 2048); xn[2] = lds_r128((xh + xo) ^ xx);
    SB();
#pragma unroll
    for (int c = 0; c < 7; ++c) {
      const bool nat = (c == 4 || c == 5);
      acc[0][c] = nat ? mma(xc[0], w[c], acc[0][c]) : mma(w[c], xc[0], acc[0][c]);
      acc[1][c] = nat ? mma(xc[1], w[c], acc[1][c]) : mma(w[c], xc[1], acc[1][c]);
      if (c == 2 + HSEL) hacc = nat ? mma(xc[2], w[c], hacc) : mma(w[c], xc[2], hacc);
      w[c] = rdW(c, wo_);
      SB();
    }
  };
  bar_raw();
  STAMP(1);
  xa[0] = lds_r128(x0); xa[1] = lds_r128(x0 + 2048); xa[2] = lds_r128(xh);
#pragma unroll
  for (int c = 0; c < 7; ++c) w[c] = rdW(c, 0);
  SB();
  u32 ws_ = 0, xs = 0;
#pragma unroll 1
  for (int k = 0; k < 9; ++k) {
    ws_ = ws_ == 4 * SLOT ? 0 : ws_ + SLOT;
    phase(xa, xb, xs, 64, ws_);
    asm volatile("" :: "v"(xb[0]), "v"(xb[1]), "v"(xb[2]));
    if (k < 8) bar_raw();
    ws_ = ws_ == 4 * SLOT ? 0 : ws_ + SLOT;
    xs = xs == 2 * SLOT ? 0 : xs + SLOT;
    phase(xb, xa, xs, 0, ws_);
  }
}

DEV void load_wo(u32x4 swo, u32 lb, int lane, int cg, int lw) {
  const int r = lane >> 3;
  const u32 vo = r * 1152 + (((lane & 7) ^ (((r >> 1) & 3) << 1)) << 4) + cg * 128 + lw * 9216;
  int src = 72 * (cg & 7);
#pragma unroll 1
  for (int n = 0; n < 72; n += 9) {
#pragma unroll
    for (int m = 0; m < 9; m += 3) dma16(swo, vo, (src + 8 * m) * 1152, lb + L_WO + (n + m + lw) * 1024);
    src = src == 504 ? 0 : src + 72;
  }
}

DEV void gemm2(DG char* ws, u16* out, u32 lb, int lane, int w, int i2, int cg, u64 tag) {
  const int g = lane >> 4, q = (lane & 15) >> 2, p = lane & 3, u = lane & 15;
  const rsrc_t rws = mk_rsrc(ws, WS_END);
  u32 bw[4], bm[4];
#pragma unroll
  for (int ct = 0; ct < 4; ++ct) {
    bw[ct] = lb + L_WO + (4 * g + q) * 128 + ((ct ^ (((4 * g + q) >> 1) & 3)) << 5) + 8 * p;
    bm[ct] = lb + L_WO + (72 * g + 16 + q) * 128 + ((ct ^ (q >> 1)) << 5) + 8 * p;
  }
  auto rdB = [&](int ks, int ct) -> u32x4 {
    if (ks < 16) { const int e = (ks & 1) << 1; return lds_tr2(bw[ct ^ e] + ks * 4608, bw[ct ^ e ^ 2] + ks * 4608 + 2560); }
    return lds_tr2(bm[ct] + (ks - 16) * 36864, bm[ct ^ 2] + (ks - 16) * 36864 + 4608);
  };
  u32x4 B[2][4];
#pragma unroll
  for (int ct = 0; ct < 4; ++ct) { B[0][ct] = rdB(0, ct); B[1][ct] = rdB(1, ct); }
  poll(ws + WS_OF + ((i2 * 4 + (w >> 1)) * 16 + (lane & 15)) * 8, tag);
  STAMP(6);
  u32x4 A[18];
  const u32 om = WS_OM + (i2 * 8 + w) * 16384;
#pragma unroll
  for (int n = 0; n < 16; ++n) A[n] = __builtin_amdgcn_raw_buffer_load_b128(rws, lane * 16, om + ((n + 2 * cg) & 15) * 1024, 16);
#pragma unroll
  for (int m = 0; m < 2; ++m) {
    const u32 vo = ((4 * m + g + cg) & 7) * 256 + u * 8;
    u32x2 a = __builtin_amdgcn_raw_buffer_load_b64(rws, vo, WS_OX + (i2 * 8 + w) * 2048, 16);
    u32x2 b = __builtin_amdgcn_raw_buffer_load_b64(rws, vo + 128, WS_OX + (i2 * 8 + w) * 2048, 16);
    A[16 + m] = u32x4{a.x, a.y, b.x, b.y};
  }
  SB();
  f32x4 acc[4] = {{0, 0, 0, 0}, {0, 0, 0, 0}, {0, 0, 0, 0}, {0, 0, 0, 0}};
#pragma unroll
  for (int ks = 0; ks < 18; ++ks) {
#pragma unroll
    for (int ct = 0; ct < 4; ++ct) {
      acc[ct] = mma(B[ks & 1][ct], A[ks], acc[ct]);
      if (ks + 2 < 18) B[ks & 1][ct] = rdB(ks + 2, ct);
    }
    SB();
  }
  STAMP(7);
  const u32 st = lb + L_W + w * 2304;
#pragma unroll
  for (int ct = 0; ct < 4; ++ct) lds_w64(st + u * 144 + ct * 32 + g * 8, pk4(acc[ct]));
  const rsrc_t ro = mk_rsrc(out, OUT_BYTES);
#pragma unroll
  for (int n = 0; n < 2; ++n) {
    const int r = 8 * n + (lane >> 3);
    u32x4 v = lds_r128(st + r * 144 + (lane & 7) * 16);
    const int row = 128 * i2 + 32 * (w & 3) + 2 * r + (w >> 2);
    __builtin_amdgcn_raw_buffer_store_b128(v, ro, row * 2304 + (lane & 7) * 16 + 1152 + cg * 128, 0, AUXB);
  }
}

DEV void role_b(DG const u16* x, const u16* wq, const u16* wo, char* ws, u32 lb, int xcd, int slot, int w, int lane) {
  const int i = 4 * (xcd >> 1) + (slot >> 2), j = 4 * (xcd & 1) + (slot & 3);
  const int cg = slot >= 9 ? slot - 9 : slot;
  const u32x4 sx = mk_srd(x, ACT_BYTES), sw = mk_srd(wq, W_BYTES);
  const int g = lane >> 4, q = (lane & 15) >> 2, p = lane & 3, u = lane & 15;
  if (w >= 9) {
    const int lw = w - 9;
    u32 vw[3];
#pragma unroll
    for (int n = 0; n < 3; ++n) {
      const int c = 64 * (3 * n + lw) + lane, row = c / 36, rem = c - row * 36, seg = rem / 12, ch = rem - seg * 12;
      vw[n] = row * 3456 + seg * 1152 + ((ch ^ ((((row >> 3) & 1) << 1) | (seg == 1))) << 4);
    }
    const int r8 = lane >> 3;
    const u32 vx0 = r8 * 2304 + (((lane & 7) ^ (r8 >> 1)) << 4), vx1 = vx0 ^ 64;
    u32 xo[3], xd[3];
#pragma unroll
    for (int n = 0; n < 3; ++n) {
      const int t = 3 * n + lw;
      xo[n] = t < 8 ? (128 * i + (t & 3) * 32 + (t >> 2)) * 1152 : (i == 0 ? 0 : (128 * i - 16) * 1152);
      xd[n] = xo[n] + (t < 8 ? 16 * 1152 : 1152);
    }
    const u32 wbase = (144 * j) & ~63;
    auto ldX = [&](int k, u32 xs) {
#pragma unroll
      for (int n = 0; n < 3; ++n) {
        dma16(sx, vx0, xo[n] + k * 128, lb + L_X + xs + (3 * n + lw) * 2048);
        dma16(sx, vx1, xd[n] + k * 128, lb + L_X + xs + (3 * n + lw) * 2048 + 1024);
      }
    };
    auto ldW = [&](int s, u32 wsl) {
#pragma unroll
      for (int hh = 0; hh < 2; ++hh)
#pragma unroll
        for (int n = 0; n < 3; ++n) dma16(sw, vw[n], (s * 32 + hh * 16) * 3456 + wbase, lb + L_W + wsl + (hh * 9 + 3 * n + lw) * 1024);
    };
    ldX(0, 0); ldW(0, 0); ldW(1, SLOT); ldX(1, SLOT);
    u32 wl = 2 * SLOT, xl = 2 * SLOT;
    WAIT_VM(6);
    STAMP(1);
#pragma unroll 1
    for (int k = 0; k < 8; ++k) {
      bar_raw();
      ldW(2 * k + 2, wl); wl = wl == 4 * SLOT ? 0 : wl + SLOT;
      ldW(2 * k + 3, wl); wl = wl == 4 * SLOT ? 0 : wl + SLOT;
      if (k < 7) { ldX(k + 2, xl); xl = xl == 2 * SLOT ? 0 : xl + SLOT; WAIT_VM(6); } else WAIT_VM(0);
    }
    bar_raw();
    STAMP(2);
    load_wo(mk_srd(wo, WO_BYTES), lb, lane, cg, lw);
    STAMP(3);
    bar_raw();
    WAIT_VM(0);
    bar_raw();
    return;
  }
  if (w == 8) {
    epoch_in(ws, lb, lane);
    const u32 off0 = (144 * j) & 63, s0 = (g & 1) << 5;
    const u32 cM = off0 + 72 * (p >> 1) + 32;
    const u32 wM = lb + L_W + (8 * g + q) * 576 + ((p & 1) ? 384 + (cM ^ s0) : 192 + (cM ^ (s0 | 16)));
    const u32 xh = lb + L_X + 16384 + u * 128 + ((g ^ (u >> 1)) << 4);
    f32x4 hacc = {0, 0, 0, 0};
    u32 wr = 0, xr = 0;
#pragma unroll 1
    for (int k = 0; k < 9; ++k) {
      bar_raw();
      u32x4 a0 = lds_r128(xh + xr), a1 = lds_r128((xh + xr) ^ 64);
      u32x4 b0 = lds_tr2(wM + wr, wM + wr + 2304);
      wr = wr == 4 * SLOT ? 0 : wr + SLOT;
      u32x4 b1 = lds_tr2(wM + wr, wM + wr + 2304);
      wr = wr == 4 * SLOT ? 0 : wr + SLOT;
      xr = xr == 2 * SLOT ? 0 : xr + SLOT;
      hacc = mma(b0, a0, hacc); hacc = mma(b1, a1, hacc);
      SB();
    }
    lds_w64(lb + L_EX + 8 * 512 + lane * 8, pk4(hacc));
    bar();
    bar_raw();
    return;
  }
  const int pi = w & 1, half = (w >> 1) & 1, pair = w >> 2;
  if (w < 2) {
    const u32 sink = lb + L_SINK;
    const int n = slot + 16 * w;
    if (n < 25) touch_unit(sink, n, xcd, sx, sw, lane * 1152, lane < 60 ? (lane / 5) * 1152 + (lane % 5) * 128 : 0x80000000u);
  }
  f32x4 acc[2][7], hacc = {0, 0, 0, 0};
#pragma unroll
  for (int c = 0; c < 7; ++c) { acc[0][c] = f32x4{0, 0, 0, 0}; acc[1][c] = f32x4{0, 0, 0, 0}; }
  switch (w & 3) {
    case 0: gemm1<0>(DA acc, hacc, lb, lane, pi, half, pair, j); break;
    case 1: gemm1<1>(DA acc, hacc, lb, lane, pi, half, pair, j); break;
    case 2: gemm1<2>(DA acc, hacc, lb, lane, pi, half, pair, j); break;
    default: gemm1<3>(DA acc, hacc, lb, lane, pi, half, pair, j); break;
  }
  STAMP(2);
  u32x2 P[2][7];
#pragma unroll
  for (int t = 0; t < 2; ++t)
#pragma unroll
    for (int c = 0; c < 7; ++c) P[t][c] = pk4(acc[t][c]);
  lds_w64(lb + L_EX + w * 512 + lane * 8, pk4(hacc));
  const u32 pkb = lb + L_EX + (9 + (pair * 2 + pi) * 5) * 512;
  if (half == 0) {
#pragma unroll
    for (int c = 2; c < 7; ++c) lds_w64(pkb + (c - 2) * 512 + lane * 8, P[1][c]);
  }
  bar();
  STAMP(9);
  const u64 tag = read_tag(lb);
  u32x2 V[5];
  {
    const u32 hk = lb + L_EX + pair * 2048 + (g * 16 + (u & 7) + 8 * pi) * 8;
    const u32 hv = lb + L_EX + pair * 2048 + 1024 + (((g & 1) + 2 * pi) * 16 + u) * 8;
    const u32 hm = lb + L_EX + 8 * 512 + ((2 * pair + (g >> 1)) * 16 + (u & 7) + 8 * pi) * 8;
    const u32 ak = half ? pkb + lane * 8 : hk, av = half ? pkb + 1024 + lane * 8 : hv, am = half ? pkb + 2048 + lane * 8 : hm;
    V[0] = lds_r64(ak); V[1] = lds_r64(ak + 512); V[2] = lds_r64(av); V[3] = lds_r64(av + 512); V[4] = lds_r64(am);
  }
  const float NEG = -__builtin_inff();
  f32x4 bC, bP, bP0;
  u32 mC[2] = {0, 0}, mP[2] = {0, 0};
  const bool first = (i == 0 && half == 0);
#pragma unroll
  for (int r = 0; r < 4; ++r) {
    const int t = 4 * g + r;
    const bool vc = t <= u && t + 7 >= u, vp = t >= u + 9;
    bC[r] = vc ? 0.f : NEG; bP[r] = vp ? 0.f : NEG; bP0[r] = (vp && !first) ? 0.f : NEG;
    mC[r >> 1] |= vc ? 0xffffu << (16 * (r & 1)) : 0u;
    mP[r >> 1] |= vp ? 0xffffu << (16 * (r & 1)) : 0u;
  }
  const u32x2 sel = {g == 3 ? (u == 0 ? 0x3f80u : u == 1 ? 0x3f800000u : 0u) : 0u, g == 3 ? (u == 2 ? 0x3f80u : u == 3 ? 0x3f800000u : 0u) : 0u};
  const f32x4 Z = {0, 0, 0, 0};
  u32x2 VX[3] = {pk4(mma16(V[4], sel, Z)), pk4(mma16(P[0][6], sel, Z)), pk4(mma16(P[1][6], sel, Z))};
  float M[4][4];
#pragma unroll
  for (int t = 0; t < 2; ++t)
#pragma unroll
    for (int hd = 0; hd < 2; ++hd) {
      u32 a = 0, m = P[t][6][hd];
      swap16(a, m);
      a = g == 1 ? a : 0u;
      const u32x4 Q = {P[t][hd].x, P[t][hd].y, a, 0u};
      const u32x4 Kc = {P[t][2 + hd].x, P[t][2 + hd].y, P[t][6][hd], 0u};
      const u32x2 kp = t ? P[0][2 + hd] : V[hd];
      const u32x4 Kp = {kp.x, kp.y, t ? P[0][6][hd] : V[4][hd], 0u};
      const f32x4 sc = mma(Kc, Q, bC), sp = mma(Kp, Q, t ? bP : bP0);
#pragma unroll
      for (int r = 0; r < 4; ++r) M[t * 2 + hd][r] = __builtin_fmaxf(sc[r], sp[r]);
    }
  float X[4], Y[4];
#pragma unroll
  for (int r = 0; r < 4; ++r) {
    swap32f(M[0][r], M[1][r]); swap32f(M[2][r], M[3][r]);
    X[r] = __builtin_fmaxf(M[0][r], M[1][r]); Y[r] = __builtin_fmaxf(M[2][r], M[3][r]);
    swap16f(X[r], Y[r]);
  }
  const float cs = 0.23570226039551584f * 1.4426950408889634f;
  const float mx = __builtin_fmaxf(__builtin_fmaxf(__builtin_fmaxf(X[0], X[1]), __builtin_fmaxf(X[2], X[3])), __builtin_fmaxf(__builtin_fmaxf(Y[0], Y[1]), __builtin_fmaxf(Y[2], Y[3])));
  const float nm = -mx * cs;
  float sum = 0.f;
#pragma unroll
  for (int r = 0; r < 4; ++r) { X[r] = __builtin_amdgcn_exp2f(__builtin_fmaf(X[r], cs, nm)); Y[r] = __builtin_amdgcn_exp2f(__builtin_fmaf(Y[r], cs, nm)); sum += X[r] + Y[r]; }
  const float rs = __builtin_amdgcn_rcpf(sum);
  u32 G[4][2];
#pragma unroll
  for (int e = 0; e < 2; ++e) {
    u32 a = pk(X[2 * e] * rs, X[2 * e + 1] * rs), b = pk(Y[2 * e] * rs, Y[2 * e + 1] * rs);
    swap16(a, b);
    u32 a2 = a, b2 = b;
    swap32(a, a2); swap32(b, b2);
    G[0][e] = a; G[1][e] = a2; G[2][e] = b; G[3][e] = b2;
  }
  const rsrc_t rws = mk_rsrc(ws, WS_END);
#pragma unroll
  for (int t = 0; t < 2; ++t) {
    f32x4 o[2], ox[2];
#pragma unroll
    for (int hd = 0; hd < 2; ++hd) {
      const u32* gg = G[t * 2 + hd];
      const u32x4 Pm = {gg[0] & mP[0], gg[1] & mP[1], gg[0] & mC[0], gg[1] & mC[1]};
      const u32x2 vp = t ? P[0][4 + hd] : V[2 + hd];
      o[hd] = mma(u32x4{vp.x, vp.y, P[t][4 + hd].x, P[t][4 + hd].y}, Pm, Z);
      ox[hd] = mma(u32x4{VX[t].x, VX[t].y, VX[t + 1].x, VX[t + 1].y}, Pm, Z);
    }
    const int rt = pi * 4 + half * 2 + t;
    const u32x2 oa = pk4(o[0]), ob = pk4(o[1]);
    __builtin_amdgcn_raw_buffer_store_b128(u32x4{oa.x, oa.y, ob.x, ob.y}, rws, lane * 16 + WS_OM + ((i * 8 + rt) * 16 + 2 * j + pair) * 1024, 0, 16);
    if (g == 0) __builtin_amdgcn_raw_buffer_store_b64(u32x2{pk(ox[0][0], ox[0][1]), pk(ox[1][2], ox[1][3])}, rws, u * 8 + WS_OX + ((i * 8 + rt) * 16 + 2 * j + pair) * 128, 0, 16);
  }
  STAMP(3);
  WAIT_VM(0);
  if (lane == 0) st_flag(ws + WS_OF + ((i * 4 + pi * 2 + half) * 16 + 2 * j + pair) * 8, tag);
  STAMP(4);
  bar_raw();
  STAMP(5);
}

DEV void role_p0(DG const u16* k, const u16* v, const u16* sk, char* ws, u32 lb, int a, int blk, int w, int lane) {
  const u32x4 sk_ = mk_srd(k, ACT_BYTES), sv = mk_srd(v, ACT_BYTES);
  const int s0 = 256 * blk + 32 * w, r8 = lane >> 3;
  const u32 st = lb + C_P0 + w * 9216;
  const u32 vo = r8 * 1152 + (((lane & 7) ^ (((r8 >> 1) & 1) << 2)) << 4);
  const u32 so = s0 * 1152 + a * 128;
  if (lane >= 32) dma16(sk_, vo, s0 ? so - 8 * 1152 : so, st);
#pragma unroll
  for (int n = 0; n < 4; ++n) { dma16(sk_, vo, so + n * 9216, st + 1024 + n * 1024); dma16(sv, vo, so + n * 9216, st + 5120 + n * 1024); }
  const u32x2 wv = *(const u32x2*)(sk + (a * 64 + lane) * 4);
  const u32 w10 = __builtin_amdgcn_alignbit(wv.x, wv.x, 16), w32 = __builtin_amdgcn_alignbit(wv.y, wv.y, 16);
  WAIT_VM(0);
  STAMP(1);
  const int g = lane >> 4, q = (lane & 15) >> 2, p = lane & 3;
  const u32 ra = st + q * 128 + ((32 * g + 8 * p) ^ ((q >> 1) << 6));
  u32 Kd[18], Vd[16];
#pragma unroll
  for (int n = 0; n < 9; ++n) { u32x2 t = lds_tr(ra + 512 + n * 512); Kd[2 * n] = t.x; Kd[2 * n + 1] = t.y; }
#pragma unroll
  for (int n = 0; n < 8; ++n) { u32x2 t = lds_tr(ra + 5120 + n * 512); Vd[2 * n] = t.x; Vd[2 * n + 1] = t.y; }
  if (s0 == 0) { Kd[0] = 0; Kd[1] = 0; }
  const rsrc_t rws = mk_rsrc(ws, WS_END);
  u32 ko = __builtin_amdgcn_alignbit(Kd[1], Kd[0], 16);
#pragma unroll
  for (int n = 0; n < 4; ++n) {
    u32x4 o;
#pragma unroll
    for (int e = 0; e < 4; ++e) {
      const int m = 4 * n + e;
      const u32 kn = __builtin_amdgcn_alignbit(Kd[m + 2], Kd[m + 1], 16);
      const float ev = dot2(kn, w10, dot2(ko, w32, 0.f)) * blo(Vd[m]);
      const float od = dot2(Kd[m + 2], w10, dot2(Kd[m + 1], w32, 0.f)) * bhi(Vd[m]);
      ko = kn;
      o[3 - e] = pk(od, ev);
    }
    const int oc = 32 * blk + 4 * w + n;
    __builtin_amdgcn_raw_buffer_store_b128(o, rws, r8 * 32768 + (((lane & 7) ^ ((oc >> 1) & 7)) << 4) + WS_KVX + a * 262144 + oc * 128, 0, 16);
  }
}

template <bool EXTRA>
DEV void role_conv(DG const u16* qp, const u16* h, const u16* Dv, u16* out, char* ws, u32 lb, int a, int sub, int w, int lane, bool didp0, int pa, int pblk) {
  const int g = lane >> 4, r = lane & 15;
  const int ch0 = a * 64 + sub * 8;
  if (w >= 8) {
    bar();
    bar_raw();
    const u32x4 sq = mk_srd(qp, ACT_BYTES);
    const int rot = EXTRA ? 2 * sub : 2 * sub;
#pragma unroll 1
    for (int n = w - 8; n < 16; n += 4) {
      const int bl = (n + rot) & 15;
      if (EXTRA) {
        dma16(sq, (lane >> 1) * 1152 + (lane & 1) * 16, (bl * 64) * 1152 + ch0 * 2, lb + C_Q + bl * 2048);
        dma16(sq, (lane >> 1) * 1152 + (lane & 1) * 16, (bl * 64 + 32) * 1152 + ch0 * 2, lb + C_Q + bl * 2048 + 1024);
      } else {
        dma16(sq, lane * 1152, (1024 + bl * 64) * 1152 + ch0 * 2, lb + C_Q + bl * 1024);
      }
    }
    WAIT_VM(0);
    bar_raw();
    return;
  }
  STAMP(9);
  u32x4 H[4];
#pragma unroll
  for (int n = 0; n < 4; ++n) {
    const int ch = EXTRA ? ch0 + 8 * (n >> 1) + w : ch0 + w, e0 = EXTRA ? (n & 1) * 512 : n * 512;
    H[n] = *(const u32x4*)(h + ch * 2048 + e0 + lane * 8);
  }
  u32 dv[2];
  dv[0] = Dv[ch0 + w]; dv[1] = EXTRA ? Dv[ch0 + 8 + w] : 0;
  bar_raw();
  const u64 tag = read_tag(lb);
  if (didp0) {
    WAIT_VM(0);
    if (lane == 0) st_flag(ws + WS_KVF + ((pa * 8 + pblk) * 8 + w) * 8, tag);
  }
  STAMP(2);
  constexpr u32 OOFF = EXTRA ? 2112 : 4160;
  const u32 hb = lb + C_H + w * HSTR;
  if (lane < (EXTRA ? 8 : 4)) lds_w128(hb + (lane >> 2) * 4192 + ((lane >> 1) & 1) * OOFF + (lane & 1) * 16, u32x4{0, 0, 0, 0});
  lds_w64(lb + C_X + (w * 64 + lane) * 8, u32x2{0, 0});
  if (EXTRA) lds_w64(lb + C_X + 20480 + (w * 64 + lane) * 8, u32x2{0, 0});
  u32 pv[4];
#pragma unroll
  for (int n = 0; n < 4; ++n) lds_w128(hb + (EXTRA ? (n >> 1) * 4192 + (n & 1) * 1024 : n * 1024) + 32 + lane * 16, H[n]);
#pragma unroll
  for (int n = 0; n < 4; ++n) pv[n] = lds_r32(hb + (EXTRA ? (n >> 1) * 4192 + (n & 1) * 1024 : n * 1024) + 28 + lane * 16);
#pragma unroll
  for (int n = 0; n < 4; ++n) {
    const u32x4 o = {__builtin_amdgcn_alignbit(H[n].x, pv[n], 16), __builtin_amdgcn_alignbit(H[n].y, H[n].x, 16), __builtin_amdgcn_alignbit(H[n].z, H[n].y, 16), __builtin_amdgcn_alignbit(H[n].w, H[n].z, 16)};
    lds_w128(hb + OOFF + (EXTRA ? (n >> 1) * 4192 + (n & 1) * 1024 : n * 1024) + 32 + lane * 16, o);
  }
  const int blk = EXTRA ? (w & 3) : w, set = EXTRA ? (w >> 2) : 0;
  poll(ws + WS_KVF + ((a * 8 + blk) * 8 + (lane & 7)) * 8, tag);
  STAMP(3);
  const rsrc_t rws = mk_rsrc(ws, WS_END);
  u32x4 xv[4];
#pragma unroll
  for (int n = 0; n < 4; ++n) xv[n] = __builtin_amdgcn_raw_buffer_load_b128(rws, lane * 16 + n * 1024, WS_KVX + (a * 8 + sub + set) * 32768 + blk * 4096, 16);
#pragma unroll
  for (int n = 0; n < 4; ++n) lds_w128(lb + C_X + set * 20480 + 4096 + blk * 4096 + n * 1024 + lane * 16, xv[n]);
  bar();
  STAMP(4);
  const u32 aa = hb + ((r & 1) ? 0 : OOFF) + ((r >> 1) + 1 + 4 * g) * 4;
  auto baddr = [&](int b) -> u32 { const int o = 33 + 2 * r - g - 4 * b; return lb + C_X + o * 128 + ((w ^ ((o >> 1) & 7)) << 4); };
  auto boff = [](int c) -> u32 { return EXTRA ? (c >> 2) * 20480 + (c & 3) * 4096 : c * 4096; };
  auto aoff = [](int c) -> u32 { return EXTRA ? (c >> 2) * 4192 + (c & 3) * 512 : c * 512; };
  constexpr int NA = EXTRA ? 8 : 4;
  f32x4 acc[NA];
  {
    const int jd = 15 - r - 8 * g;
    const u32 b0 = baddr(0);
#pragma unroll
    for (int t = 0; t < NA; ++t) {
      const u32 d = dv[EXTRA ? t >> 2 : 0] << (16 * (jd & 1));
      const u32x4 AD = {(jd >> 1) == 0 ? d : 0u, (jd >> 1) == 1 ? d : 0u, (jd >> 1) == 2 ? d : 0u, (jd >> 1) == 3 ? d : 0u};
      acc[t] = mma(AD, lds_r128(b0 + boff(EXTRA ? t : 4 + t)), f32x4{0, 0, 0, 0});
    }
  }
  u32x4 A[8], B[8];
  {
    const u32 ba = baddr(0);
#pragma unroll
    for (int c = 0; c < 8; ++c) { const int cb = EXTRA ? c ^ 3 : 7 - c; A[c] = lds_r4x32(aa + aoff(c)); B[cb] = lds_r128(ba + boff(cb)); }
  }
  SB();
#pragma unroll 1
  for (int b = 1; b < 9; ++b) {
    const u32 ba = baddr(b & 7), ab = aa + 64 * (b & 7);
#pragma unroll
    for (int c = 0; c < 8; ++c) {
      if (EXTRA) {
#pragma unroll
        for (int t = (c | 3); t >= c; --t) acc[t] = mma(A[c], B[(c & 4) + t - c], acc[t]);
      } else {
#pragma unroll
        for (int t = 7; t >= (c > 4 ? c : 4); --t) acc[t - 4] = mma(A[c], B[t - c], acc[t - 4]);
      }
      SB();
      const int cb = EXTRA ? c ^ 3 : 7 - c;
      A[c] = lds_r4x32(ab + aoff(c)); B[cb] = lds_r128(ba + boff(cb));
      SB();
    }
  }
  STAMP(5);
#pragma unroll
  for (int t = 0; t < NA; ++t) lds_w64(hb + t * 512 + r * 32 + g * 8, pk4(acc[t]));
  bar();
  STAMP(6);
  const rsrc_t ro = mk_rsrc(out, OUT_BYTES);
  const int q = r >> 2, p = lane & 3;
#pragma unroll
  for (int n = 0; n < 2; ++n)
#pragma unroll
    for (int s = 0; s < (EXTRA ? 2 : 1); ++s) {
      const int row = w * 128 + n * 64;
      const u32 za = lb + C_H + q * HSTR + s * 2048 + (row + 16 * g) * 2 + 8 * p;
      const u32x4 z = lds_tr2(za, za + 4 * HSTR);
      const u32x4 qq = lds_r128(lb + C_Q + (EXTRA ? (row + lane) * 32 + s * 16 : (row + lane) * 16));
      u32x4 o;
#pragma unroll
      for (int e = 0; e < 4; ++e) o[e] = pk(blo(z[e]) * blo(qq[e]), bhi(z[e]) * bhi(qq[e]));
      __builtin_amdgcn_raw_buffer_store_b128(o, ro, lane * 2304 + ((EXTRA ? 0 : 1024) + row) * 2304 + ch0 * 2 + s * 16, 0, AUXA);
    }
}

__global__ __launch_bounds__(NT) void fused_kernel(const u16* __restrict__ q, const u16* __restrict__ k, const u16* __restrict__ v,
                                                   const u16* __restrict__ sk, const u16* __restrict__ h, const u16* __restrict__ Dv,
                                                   const u16* __restrict__ x, const u16* __restrict__ wq, const u16* __restrict__ wo,
                                                   u16* __restrict__ out, char* __restrict__ ws) {
  __shared__ __attribute__((aligned(16))) char lds[LDS_BYTES];
  const u32 lb = (u32)(size_t)(LAS char*)lds;
  const int xcd = blockIdx.x & 7, slot = blockIdx.x >> 3;
  const int w = __builtin_amdgcn_readfirstlane(threadIdx.x >> 6), lane = threadIdx.x & 63;
  STAMP(0);
  if (slot < 16) {
    role_b(DA x, wq, wo, ws, lb, xcd, slot, w, lane);
  } else {
  const bool p0 = slot <= 24;
  if (w >= 8) {
    if (w == 8) epoch_in(ws, lb, lane);
  } else if (p0) {
    role_p0(DA k, v, sk, ws, lb, slot - 16, xcd, w, lane);
  } else if (w == 0) {
    const u32 sink = lb + L_SINK;
    const u32x4 sx = mk_srd(x, ACT_BYTES), sw = mk_srd(wq, W_BYTES), so = mk_srd(wo, WO_BYTES);
    const u32 vx = lane * 1152, vw = lane < 60 ? (lane / 5) * 1152 + (lane % 5) * 128 : 0x80000000u;
#pragma unroll 1
    for (int n = 25 + slot - 25; n < 225; n += 7) touch_unit(sink, n, xcd, sx, sw, vx, vw);
#pragma unroll 1
    for (int n = slot - 25; n < 81; n += 7) touch(sink, so, lane * 128, n * 8192);
    WAIT_VM(0);
    STAMP(1);
  }
  const bool ninth = slot == 24 || slot == 25;
  const int ca = ninth ? 8 : xcd;
  if (slot < 24 || (ninth && xcd < 4)) role_conv<false>(DA q, h, Dv, out, ws, lb, ca, ninth ? 2 * xcd + slot - 24 : slot - 16, w, lane, p0, slot - 16, xcd);
  else if (slot < 30 && (!ninth || xcd < 6)) role_conv<true>(DA q, h, Dv, out, ws, lb, ca, ninth ? 4 * (xcd - 4) + 2 * (slot - 24) : 2 * (slot - 26), w, lane, p0, slot - 16, xcd);
  else {
    if (slot >= 30 && w >= 9) load_wo(mk_srd(wo, WO_BYTES), lb, lane, slot - 23, w - 9);
    WAIT_VM(0);
    bar();
    const u64 tag = read_tag(lb);
    if (p0) {
      if (w < 8 && lane == 0) st_flag(ws + WS_KVF + ((8 * 8 + xcd) * 8 + w) * 8, tag);
    } else if (slot == 25 && xcd == 7 && w == 0) {
#pragma unroll 1
      for (int n = 0; n < 4; ++n) poll(ws + WS_CHECK + (n * 64 + lane) * 8, tag);
      if (lane == 0) st_flag(ws + WS_EPOCH, tag);
    }
  }
  }
  if ((slot < 16 || slot >= 30) && w < 8) gemm2(DA ws, out, lb, lane, w, 2 * xcd + (slot >= 9), slot < 9 ? slot : slot < 16 ? slot - 9 : slot - 23, read_tag(lb));
  STAMP(8);
}

extern "C" void kernel_launch(void* const* d_in, const int* in_sizes, int n_in, void* d_out, int out_size, void* d_ws, size_t ws_size,
                              hipStream_t stream) {
  fused_kernel<<<256, NT, 0, stream>>>((const u16*)d_in[0], (const u16*)d_in[1], (const u16*)d_in[2], (const u16*)d_in[3], (const u16*)d_in[4],
                                       (const u16*)d_in[5], (const u16*)d_in[6], (const u16*)d_in[7], (const u16*)d_in[8], (u16*)d_out, (char*)d_ws);
}
```

```cpp
#include <hip/hip_runtime.h>
#include <hip/hip_bf16.h>
#pragma clang diagnostic ignored "-Wint-to-pointer-cast"

typedef unsigned u32;
typedef unsigned short u16;
typedef unsigned long long u64;
typedef __attribute__((ext_vector_type(4))) u32 u32x4;
typedef __attribute__((ext_vector_type(2))) u32 u32x2;
typedef __attribute__((ext_vector_type(4))) float f32x4;
typedef __attribute__((ext_vector_type(2))) float f32x2;
typedef __attribute__((ext_vector_type(8))) __bf16 bf16x8;
typedef __attribute__((ext_vector_type(2))) __bf16 bf16x2;
typedef __attribute__((ext_vector_type(4))) short s16x4;
#define LAS __attribute__((address_space(3)))
#define GAS __attribute__((address_space(1)))
#define DEV __device__ __forceinline__
typedef __amdgpu_buffer_rsrc_t rsrc_t;

constexpr int NT = 768, NWV = 12;
constexpr u32 ACT_BYTES = 2048u * 1152u;
constexpr u32 W_BYTES = 576u * 3456u;
constexpr u32 WO_BYTES = 576u * 1152u;
constexpr u32 OUT_BYTES = 2048u * 2304u;
constexpr u32 WS_EPOCH = 0, WS_CHECK = 4096, WS_KVF = 8192;
constexpr u32 WS_KVX = 131072, WS_OM = WS_KVX + 2359296, WS_OX = WS_OM + 2097152, WS_END = WS_OX + 262144;
constexpr u32 WS_DIAG = 8u << 20;
constexpr u32 SLOT = 18432, WSLOT = 18560, WK = 6144 + 16, WV = 2 * 6144 + 16 + 8;
constexpr u32 L_W = 0, L_X = 5 * WSLOT, L_WO = 3 * WSLOT, L_EX = L_X + 3 * SLOT;
constexpr u32 L_OX = WSLOT;
constexpr u32 E_HX = L_EX + 4096, E_MX = E_HX + 512, ESTR = 528;
constexpr u32 L_TAG = E_MX + 16 * ESTR;
constexpr u32 L_SINK = L_TAG + 16;
constexpr u32 LDS_BYTES = L_SINK + 256;
constexpr u32 HSTR = 8384;
constexpr u32 C_H = 0, C_X = 8 * HSTR, C_Q = C_X + 40960, C_P0 = C_X;
static_assert(C_P0 + 8 * 9216 <= L_TAG && C_Q + 32768 <= L_TAG, "lds");
constexpr u32 SENT = 0xffffffffu;
constexpr int SPIN_LIMIT = 40000;
#define ABL 0
#define AUXA 0
#define AUXB 0
#define CODE_UNITS 6
#define KGAP 2
#define DGAP 6
#define G2STAMP 0
#define CLRSTEP 15
#define ROTM 15
#define ROT(c) ((c) & ROTM)
#define XDELAY 8
#define NB 25
#define NP 4
#define PFW 2
#define GDA 6
#define GDB 4
#define F3STEP 12
#define PGAP 3
#define NH 6
#define RPRIO 0
#define PACE 0
#define NFAST 8
#define NE 22
#define CSLEEP 80
#define CLR_FIRST 1

#define STAMP(n) do {} while (0)
#define DG
#define DA

DEV u32 pk(float a, float b) { f32x2 v = {a, b}; return __builtin_bit_cast(u32, __builtin_convertvector(v, bf16x2)); }
DEV float blo(u32 x) { return __uint_as_float(x << 16); }
DEV float bhi(u32 x) { return __uint_as_float(x & 0xffff0000u); }
DEV u32x2 lds_tr(u32 a) { return __builtin_bit_cast(u32x2, __builtin_amdgcn_ds_read_tr16_b64_v4i16((LAS s16x4*)a)); }
DEV u32x4 lds_tr2(u32 a, u32 b) { u32x2 x = lds_tr(a), y = lds_tr(b); return u32x4{x.x, x.y, y.x, y.y}; }
DEV u32x4 lds_r128(u32 a) { return *(const LAS u32x4*)a; }
DEV u32x2 lds_r64(u32 a) { return *(const LAS u32x2*)a; }
DEV u32x2 lds_r64v(u32 a) { return *(const volatile LAS u32x2*)a; }
DEV u32 lds_r32(u32 a) { return *(const LAS u32*)a; }
DEV u32x4 lds_r4x32(u32 a) { const LAS u32* p = (const LAS u32*)a; return u32x4{p[0], p[1], p[2], p[3]}; }
DEV void lds_w128(u32 a, u32x4 v) { *(LAS u32x4*)a = v; }
DEV void lds_w64(u32 a, u32x2 v) { *(LAS u32x2*)a = v; }
DEV f32x4 mma(u32x4 a, u32x4 b, f32x4 c) { return __builtin_amdgcn_mfma_f32_16x16x32_bf16(__builtin_bit_cast(bf16x8, a), __builtin_bit_cast(bf16x8, b), c, 0, 0, 0); }
DEV f32x4 mma16(u32x2 a, u32x2 b, f32x4 c) { return __builtin_amdgcn_mfma_f32_16x16x16bf16_1k(__builtin_bit_cast(s16x4, a), __builtin_bit_cast(s16x4, b), c, 0, 0, 0); }
DEV float dot2(u32 a, u32 b, float c) { return __builtin_amdgcn_fdot2_f32_bf16(__builtin_bit_cast(bf16x2, a), __builtin_bit_cast(bf16x2, b), c, false); }
DEV u32x2 pk4(f32x4 v) { return u32x2{pk(v[0], v[1]), pk(v[2], v[3])}; }
DEV void swap16(u32& a, u32& b) { auto r = __builtin_amdgcn_permlane16_swap(a, b, false, false); a = r[0]; b = r[1]; }
DEV void swap32(u32& a, u32& b) { auto r = __builtin_amdgcn_permlane32_swap(a, b, false, false); a = r[0]; b = r[1]; }
DEV void swap16f(float& a, float& b) { u32 x = __float_as_uint(a), y = __float_as_uint(b); swap16(x, y); a = __uint_as_float(x); b = __uint_as_float(y); }
DEV void swap32f(float& a, float& b) { u32 x = __float_as_uint(a), y = __float_as_uint(b); swap32(x, y); a = __uint_as_float(x); b = __uint_as_float(y); }
DEV float fmx3(float a, float b, float c) { float r; asm("v_max3_f32 %0, %1, %2, %3" : "=v"(r) : "v"(a), "v"(b), "v"(c)); return r; }
DEV u32 lsel(u64 m, u32 a, u32 b) { return __builtin_amdgcn_inverse_ballot_w64(m) ? a : b; }
DEV float lself(u64 m, float a, float b) { return __builtin_amdgcn_inverse_ballot_w64(m) ? a : b; }
constexpr u64 LM_G1 = 0xffff0000ull;
constexpr u64 lm_prev(int r) { u64 m = 0; for (int l = 0; l < 64; ++l) if (4 * (l >> 4) + r >= (l & 15) + 9) m |= 1ull << l; return m; }
constexpr u64 lm_low(int r) { u64 m = 0; for (int l = 0; l < 64; ++l) { const int t = 4 * ((l >> 4) & 1) + r, u = l & 15; if (t <= u && t + 7 >= u) m |= 1ull << l; } return m; }
DEV void bar() { asm volatile("s_waitcnt lgkmcnt(0)\n\ts_barrier" ::: "memory"); }
DEV void bar_raw() { asm volatile("s_barrier" ::: "memory"); }
#define BK1() do { if (!(ABL & 4)) bar_raw(); } while (0)
#define LD(x) if (!(ABL & 1)) { x }
#define SB() __builtin_amdgcn_sched_barrier(0)
#define WAIT_VM(n) asm volatile("s_waitcnt vmcnt(%0)" :: "n"(n) : "memory")

DEV u32x4 mk_srd(const void* p, u32 bytes) { u64 a = (u64)p; return u32x4{(u32)a, (u32)(a >> 32) & 0xffffu, bytes, 0x20000u}; }
DEV rsrc_t mk_rsrc(const void* p, u32 bytes) { return __builtin_amdgcn_make_buffer_rsrc((void*)p, 0, bytes, 0x20000); }
DEV void dma16(u32x4 srd, u32 voff, u32 soff, u32 lds) {
  asm volatile("s_mov_b32 m0, %3\n\ts_nop 0\n\tbuffer_load_dwordx4 %0, %1, %2 offen lds" :: "v"(voff), "s"(srd), "s"(soff), "s"(lds) : "memory");
}
template <int SO, int LO>
DEV void dma16i(u32x4 srd, u32 voff, u32 soff, u32 lds) {
  u32 t;
  asm volatile("s_add_u32 m0, %4, %c6\n\ts_add_u32 %0, %3, %c5\n\tbuffer_load_dwordx4 %1, %2, %0 offen lds"
               : "=&s"(t) : "v"(voff), "s"(srd), "s"(soff), "s"(lds), "i"(SO), "i"(LO) : "memory", "scc");
}
DEV void touch(u32 sink, u32x4 srd, u32 voff, u32 soff) {
  if (ABL & 8) return;
  asm volatile("s_mov_b32 m0, %3\n\ts_nop 0\n\tbuffer_load_dword %0, %1, %2 offen lds" :: "v"(voff), "s"(srd), "s"(soff), "s"(sink) : "memory");
}
DEV u64 ld_flag(const char* p) { return __hip_atomic_load((const GAS u64*)p, __ATOMIC_RELAXED, __HIP_MEMORY_SCOPE_AGENT); }
DEV void st_flag(char* p, u64 v) { __hip_atomic_store((GAS u64*)p, v, __ATOMIC_RELAXED, __HIP_MEMORY_SCOPE_AGENT); }
template <int SLEEP>
DEV void poll(const char* p, u64 tag) {
#pragma unroll 1
  for (int n = 0; n < SPIN_LIMIT; ++n) {
    if (__all(ld_flag(p) == tag)) break;
    if (SLEEP) __builtin_amdgcn_s_sleep(SLEEP);
  }
  asm volatile("" ::: "memory");
}
struct Args { const u16 *q, *k, *v, *sk, *h, *Dv, *x, *wq, *wo; u16* out; char* ws; };
typedef const __attribute__((address_space(4))) Args* argp;
#define FIN() __builtin_amdgcn_endpgm()
constexpr int NLATE = NB + 7 * NE + 9 * NP + 2 * NH < 225 ? NB + 7 * NE + 9 * NP + 2 * NH : 225;
DEV void touch_unit(u32 sink, int n, int xcd, u32x4 sx, u32x4 sw, u32 vx, u32 vw) {
  int kc = n / 25, r = n - kc * 25;
  if (r < 9) {
    int row0 = 512 * (xcd >> 1) + (r < 8 ? 64 * r : -64);
    if (row0 >= 0) touch(sink, sx, r < 8 ? vx : (vx < 48 * 1152 ? 0x80000000u : vx), row0 * 1152 + kc * 128);
  } else {
    touch(sink, sw, vw, (kc * 192 + (r - 9) * 12) * 1152 + (xcd & 1) * 512);
  }
}

template <int ODD>
DEV void gemm1(DG f32x4 (&acc)[3][4], f32x4 (&mac)[2], u32 wA, u32 wM, u32 x0, rsrc_t rws, u32 o0, u32 o1, u32 o2) {
  u32x4 sent = {SENT, SENT, SENT, SENT};
  asm volatile("" : "+v"(sent));
  u32x2 wl[4], wh[4];
  u32x4 xf[2][4];
  u32 wA2 = wA + 3 * WSLOT, wM2 = wM + 3 * WSLOT, x1 = x0 ^ 64;
  asm volatile("" : "+v"(wA2), "+v"(wM2), "+v"(x1));
  auto wadr = [&](int st, int c) -> u32 {
    const int sl = st % 5;
    return (c == 3 ? (sl < 3 ? wM : wM2) : (sl < 3 ? wA : wA2) + (c == 0 ? 0 : c == 1 ? WK : WV)) + (sl % 3) * WSLOT;
  };
  auto xadr = [&](int st, int t) -> u32 { return ((st & 1) ? x1 : x0) + ((st >> 1) % 3) * SLOT + t * 2048; };
  bar_raw();
  STAMP(1);
  wl[0] = lds_tr(wadr(0, 0)); wh[0] = lds_tr(wadr(0, 0) + 768);
#pragma unroll
  for (int t = 0; t < 4; ++t) xf[0][t] = lds_r128(xadr(0, t));
  SB();
#pragma unroll
  for (int c = 1; c < 3; ++c) { wl[c] = lds_tr(wadr(0, c)); wh[c] = lds_tr(wadr(0, c) + 768); SB(); }
  const f32x4 Z = {0, 0, 0, 0};
#pragma unroll
  for (int st = 0; st < 18; ++st) {
#pragma unroll
    for (int n = 0; n < 14; ++n) {
      const int c = n >> 2, t = c == 3 ? 2 * ODD + (n & 1) : (n & 3);
      const u32x4 wv = {wl[c].x, wl[c].y, wh[c].x, wh[c].y}, xv = xf[st & 1][t];
      if ((ABL & 2) && st) {} else if (c == 3) mac[n & 1] = mma(wv, xv, st ? mac[n & 1] : Z);
      else acc[c][t] = c == 2 ? mma(xv, wv, st ? acc[c][t] : Z) : mma(wv, xv, st ? acc[c][t] : Z);
      SB();
      if (n == 0) wl[3] = lds_tr(wadr(st, 3));
      if (n == 1) wh[3] = lds_tr(wadr(st, 3) + 768);
      if (st < 17) {
        if (n == 3) {
          if (!(ABL & 4)) bar_raw();
          if (st == CLRSTEP) {
            __builtin_amdgcn_raw_buffer_store_b128(sent, rws, o0, 0, 16);
            __builtin_amdgcn_raw_buffer_store_b128(sent, rws, o1, 0, 16);
            __builtin_amdgcn_raw_buffer_store_b32(sent.x, rws, o2, 0, 16);
          }
        }
        if (n == 4) wl[0] = lds_tr(wadr(st + 1, 0));
        if (n == 5) wh[0] = lds_tr(wadr(st + 1, 0) + 768);
        if (n >= 6 && n < 10) xf[(st + 1) & 1][n - 6] = lds_r128(xadr(st + 1, n - 6));
        if (n == 10 || n == 12) wl[(n - 8) >> 1] = lds_tr(wadr(st + 1, (n - 8) >> 1));
        if (n == 11 || n == 13) wh[(n - 8) >> 1] = lds_tr(wadr(st + 1, (n - 8) >> 1) + 768);
      }
      SB();
    }
  }
}

DEV void load_wo(u32x4 swo, u32 lb, int lane, int cg, int bk) {
  const int r = lane >> 3;
  const u32 vo = r * 1152 + (((lane & 7) ^ (((r >> 1) & 3) << 1)) << 4) + cg * 128;
  const u32 src = ((bk + ROT(cg)) & 7) * 72 * 1152;
  const u32 dst = lb + L_WO + bk * 9216;
  dma16i<0, 0>(swo, vo, src, dst); dma16i<9216, 1024>(swo, vo, src, dst); dma16i<18432, 2048>(swo, vo, src, dst);
  dma16i<27648, 3072>(swo, vo, src, dst); dma16i<36864, 4096>(swo, vo, src, dst); dma16i<46080, 5120>(swo, vo, src, dst);
  dma16i<55296, 6144>(swo, vo, src, dst); dma16i<64512, 7168>(swo, vo, src, dst); dma16i<73728, 8192>(swo, vo, src, dst);
}
struct Relay { u32 r[4], w; };
DEV Relay relay_adr(u32 lb, int lane, int bk) {
  const int g = lane >> 4, q = (lane & 15) >> 2, p = lane & 3, row = 20 * (g >> 1) + 4 * (g & 1) + q;
  Relay R;
#pragma unroll
  for (int ct = 0; ct < 4; ++ct) R.r[ct] = lb + L_WO + bk * 9216 + row * 128 + ((ct ^ ((row >> 1) & 3)) << 5) + 8 * p;
  R.w = lb + L_WO + bk * 9216 + lane * 16;
  asm volatile("" : "+v"(R.r[0]), "+v"(R.r[1]), "+v"(R.r[2]), "+v"(R.r[3]), "+v"(R.w));
  return R;
}
DEV void relay_wo(const Relay& R, u32 off) {
  u32x4 f[2][4];
#pragma unroll
  for (int e = 0; e < 2; ++e)
#pragma unroll
    for (int ct = 0; ct < 4; ++ct) f[e][ct] = lds_tr2(R.r[ct ^ (2 * e)] + off + e * 4608, R.r[ct ^ (2 * e)] + off + e * 4608 + 1024);
#pragma unroll
  for (int e = 0; e < 2; ++e)
#pragma unroll
    for (int ct = 0; ct < 4; ++ct) lds_w128(R.w + off + e * 4608 + ct * 1024 + (ct >> 1) * 512, f[e][ct]);
}
DEV void gemm2_aux(DG char* ws, u32 lb, int lane, int w, int i2, int nap) {
  const rsrc_t rws = mk_rsrc(ws + WS_OX + i2 * 16384, 16384);
  bar();
  __builtin_amdgcn_s_setprio(3);
  if (w == 8) {
    if (nap) {
#pragma unroll 1
      for (int it = 0; it < SPIN_LIMIT; ++it) {
        asm volatile("" ::: "memory");
        if (__builtin_amdgcn_raw_buffer_load_b32(rws, 252, 0, 16) != SENT) break;
        __builtin_amdgcn_s_sleep(4);
      }
    }
    u32 a = __builtin_amdgcn_raw_buffer_load_b32(rws, lane * 256 + 252, 0, 16);
#pragma unroll 1
    for (int it = 0; it < SPIN_LIMIT; ++it) {
      __builtin_amdgcn_s_sleep(PGAP);
      asm volatile("" ::: "memory");
      const u32 b = __builtin_amdgcn_raw_buffer_load_b32(rws, lane * 256 + 252, 0, 16);
      if (__all(a != SENT)) break;
      a = b;
    }
  }
  bar_raw();
  if (XDELAY) __builtin_amdgcn_s_sleep(XDELAY);
  u32x4 v[4];
#pragma unroll 1
  for (int it = 0; it < SPIN_LIMIT; ++it) {
    u32 chk = 0;
    asm volatile("" ::: "memory");
#pragma unroll
    for (int n = 0; n < 4; ++n) v[n] = __builtin_amdgcn_raw_buffer_load_b128(rws, (w - 8) * 4096 + lane * 16 + n * 1024, 0, 16);
#pragma unroll
    for (int n = 0; n < 4; ++n) chk = max(max(max(chk, v[n].x), max(v[n].y, v[n].z)), v[n].w);
    if (__all(chk != SENT)) break;
  }
#pragma unroll
  for (int n = 0; n < 4; ++n) lds_w128(lb + L_OX + (w - 8) * 4096 + n * 1024 + lane * 16, v[n]);
  bar();
}

DEV void gemm2(DG char* ws, u16* out, u32 lb, int lane, int w, int i2, int cg) {
  const int g = lane >> 4, q = (lane & 15) >> 2, p = lane & 3, u = lane & 15;
  const rsrc_t rws = mk_rsrc(ws + WS_OM + (i2 * 8 + w) * 16384, 16384);
  u32 bw = lb + L_WO + lane * 16, bw2 = bw + 8 * 4608;
  u32 ax0 = lb + L_OX + (w >> 2) * 8192 + ((g + ROT(cg)) & 7) * 1024 + (w & 3) * 64 + u * 4;
  u32 ax1 = lb + L_OX + (w >> 2) * 8192 + ((g + ROT(cg) + 4) & 7) * 1024 + (w & 3) * 64 + u * 4;
  asm volatile("" : "+v"(bw), "+v"(bw2), "+v"(ax0), "+v"(ax1));
  u32 so[8];
#pragma unroll
  for (int n = 0; n < 8; ++n) { so[n] = ((n + ROT(cg)) & 7) * 2048; asm volatile("" : "+s"(so[n])); }
  auto rdB = [&](int ks, int ct) -> u32x4 { return lds_r128((ks < 8 ? bw : bw2) + (ks & 7) * 4608 + ct * 1024 + (ct >> 1) * 512); };
  auto ldA = [&](int n) -> u32x4 { return __builtin_amdgcn_raw_buffer_load_b128(rws, lane * 16 + (n & 1) * 1024, so[n >> 1], 16); };
  constexpr int DA_ = GDA, DB = GDB;
  u32x4 BM[2][4], B[DB][4], A[DA_];
  u32 bm[4];
#pragma unroll
  for (int ct = 0; ct < 4; ++ct) { bm[ct] = lb + L_WO + (72 * g + 16 + q) * 128 + 8 * p + ((ct ^ (q >> 1)) << 5); asm volatile("" : "+v"(bm[ct])); }
  u32 sw_ = lb + L_W + w * 2304 + u * 144 + g * 8, sr_ = lb + L_W + w * 2304 + (lane >> 3) * 144 + (lane & 7) * 16;
  u32 oa = (128 * i2 + 32 * (w & 3) + 2 * (lane >> 3) + (w >> 2)) * 2304 + (lane & 7) * 16 + 1152 + cg * 128, ob = oa + 16 * 2304;
  const rsrc_t ro = mk_rsrc(out, OUT_BYTES);
  asm volatile("" : "+v"(sw_), "+v"(sr_), "+v"(oa), "+v"(ob));
  bar_raw();
  STAMP(5);
#pragma unroll
  for (int m = 0; m < 2; ++m)
#pragma unroll
    for (int ct = 0; ct < 4; ++ct) BM[m][ct] = lds_tr2(bm[ct] + m * 36864, bm[ct ^ 2] + m * 36864 + 4608);
#pragma unroll
  for (int ks = 0; ks < DB; ++ks)
#pragma unroll
    for (int ct = 0; ct < 4; ++ct) B[ks][ct] = rdB(ks, ct);
  SB();
  bar_raw();
  STAMP(6);
  f32x4 acc[4];
  u32x4 am, an;
#pragma unroll
  for (int n = 0; n < DA_; ++n) A[n] = ldA(n);
  SB();
  u32 chk = 0;
#pragma unroll
  for (int ks = 0; ks < 16; ++ks)
#pragma unroll
    for (int ct = 0; ct < 4; ++ct) {
      acc[ct] = mma(B[ks % DB][ct], A[ks % DA_], ks ? acc[ct] : f32x4{0, 0, 0, 0});
      SB();
      if (ks + DB < 16) B[ks % DB][ct] = rdB(ks + DB, ct);
      if (ct < 2) chk = max(max(chk, A[ks % DA_][2 * ct]), A[ks % DA_][2 * ct + 1]);
      if (ct == 3 && ks + DA_ < 16) A[ks % DA_] = ldA(ks + DA_);
      if (ks == F3STEP && ct == 3) bar_raw();
      if (G2STAMP && ks == 0 && ct == 0) STAMP(12);
      if (G2STAMP && ks == 8 && ct == 0) STAMP(13);
      if (ks == 14 && ct == 2) { const LAS u32* pa = (const LAS u32*)ax0; am = u32x4{pa[0], pa[64], pa[128], pa[192]}; }
      if (ks == 14 && ct == 3) { const LAS u32* pb = (const LAS u32*)ax1; an = u32x4{pb[0], pb[64], pb[128], pb[192]}; }
      SB();
    }
  if (__builtin_expect(!__all(chk != SENT), 0)) {
#pragma unroll
    for (int ct = 0; ct < 4; ++ct) acc[ct] = f32x4{0, 0, 0, 0};
#pragma unroll 1
    for (int ks = 0; ks < 16; ++ks) {
      u32x4 a;
#pragma unroll 1
      for (int it = 0; it < SPIN_LIMIT; ++it) {
        asm volatile("" ::: "memory");
        a = __builtin_amdgcn_raw_buffer_load_b128(rws, lane * 16, ((ks + 2 * ROT(cg)) & 15) * 1024, 16);
        if (__all(max(max(a.x, a.y), max(a.z, a.w)) != SENT)) break;
      }
#pragma unroll
      for (int ct = 0; ct < 4; ++ct) acc[ct] = mma(lds_r128(bw + ks * 4608 + ct * 1024 + (ct >> 1) * 512), a, acc[ct]);
    }
  }
#pragma unroll
  for (int ct = 0; ct < 4; ++ct) acc[ct] = mma(BM[0][ct], am, acc[ct]);
#pragma unroll
  for (int ct = 0; ct < 4; ++ct) acc[ct] = mma(BM[1][ct], an, acc[ct]);
  STAMP(7);
#pragma unroll
  for (int ct = 0; ct < 4; ++ct) lds_w64(sw_ + ct * 32, pk4(acc[ct]));
  const u32x4 v0 = lds_r128(sr_), v1 = lds_r128(sr_ + 8 * 144);
  __builtin_amdgcn_raw_buffer_store_b128(v0, ro, oa, 0, AUXB);
  __builtin_amdgcn_raw_buffer_store_b128(v1, ro, ob, 0, AUXB);
}

template <bool LOADER>
DEV u64 b_aux(DG argp A, u32 lb, int xcd, int slot, int cg, int i2, int w, int lane) {
  const int i = 4 * (xcd >> 1) + (slot >> 2), j = 4 * (xcd & 1) + (slot & 3);
  const int g = lane >> 4, q = (lane & 15) >> 2, p = lane & 3, u = lane & 15;
  const int hd = w & 3;
  const u32 off0 = (144 * j) & 63, rowb = lb + L_W + (8 * g + q) * 192, s0 = (g & 1) << 5;
  const u32 wA = rowb + ((off0 + 72 * (hd >> 1) + 40 * (hd & 1) + 8 * p) ^ s0);
  const u32 xr = lb + L_X + u * 128 + ((g ^ (u >> 1)) << 4);
  u64 tag = 0;
  {
    __builtin_amdgcn_s_setprio(3);
    f32x4 hk = {0, 0, 0, 0}, hv = hk, hm = hk;
    u32x4 a = {0, 0, 0, 0}, bk = a, bv = a;
    u32 wr = 0, xs = 0;
    auto halo = [&](int odd) {
      hk = mma(bk, a, hk); hv = mma(a, bv, hv);
      SB();
      a = lds_r128(((xr + 16384) ^ (odd * 64)) + xs);
      bk = lds_tr2(wA + wr + WK, wA + wr + WK + 768); bv = lds_tr2(wA + wr + WV, wA + wr + WV + 768);
      SB();
    };
    auto adv = [&](int odd) { wr = wr == 4 * WSLOT ? 0 : wr + WSLOT; if (odd) xs = xs == 2 * SLOT ? 0 : xs + SLOT; };
    u32x4 swo;
    if (LOADER) {
      const u16* x = A->x;
      const u32x4 sw = mk_srd(A->wq, W_BYTES);
      const int lw = w - 9, r8 = lane >> 3;
      u32 vx[2][3], vw[3];
      const u32 vx0 = r8 * 2304 + (((lane & 7) ^ (r8 >> 1)) << 4);
#pragma unroll
      for (int n = 0; n < 3; ++n) {
        const int t = 3 * n + lw;
        const int hl = t >> 3;
        const u32 ro = (16 + (t & 3) * 32 + ((t >> 2) & 1) - 16 * hl * (i != 0)) * 1152;
        vx[0][n] = vx0 + ro; vx[1][n] = (vx0 ^ 64) + ro + (16 - 15 * hl) * 1152;
      }
      const u32x4 sxt = mk_srd((const char*)x + (128 * i - 16) * 1152, ACT_BYTES - (128 * i - 16) * 1152);
      u32 xk = 0, xl = lb + L_X + lw * 2048;
      auto ldX = [&](int hf) {
        if (hf == 0) { dma16i<0, 0>(sxt, vx[0][0], xk, xl); dma16i<0, 6144>(sxt, vx[0][1], xk, xl); dma16i<0, 12288>(sxt, vx[0][2], xk, xl); }
        else {
          dma16i<0, 1024>(sxt, vx[1][0], xk, xl); dma16i<0, 7168>(sxt, vx[1][1], xk, xl); dma16i<0, 13312>(sxt, vx[1][2], xk, xl);
          xk += 128; xl = xl >= lb + L_X + 2 * SLOT ? xl - 2 * SLOT : xl + SLOT;
        }
      };
      STAMP(5);
      ldX(0); ldX(1);
#pragma unroll
      for (int n = 0; n < 3; ++n) {
        const int c = 64 * n + lane, row = c / 12, ch = c - row * 12;
        vw[n] = row * 3456 + ((ch ^ ((row >> 3) << 1)) << 4);
      }
      u32 wk = ((144 * j) & ~63) + lw * 1152, wl = lb + L_W + (lw == 0 ? 0 : lw == 1 ? WK : WV);
      auto ldW = [&]() {
        dma16i<0, 0>(sw, vw[0], wk, wl); dma16i<0, 1024>(sw, vw[1], wk, wl); dma16i<0, 2048>(sw, vw[2], wk, wl);
        dma16i<55296, 3072>(sw, vw[0], wk, wl); dma16i<55296, 4096>(sw, vw[1], wk, wl); dma16i<55296, 5120>(sw, vw[2], wk, wl);
        wk += 32 * 3456; wl = wl >= lb + L_W + 4 * WSLOT ? wl - 4 * WSLOT : wl + WSLOT;
      };
      ldW(); ldW(); ldX(0);
      STAMP(6);
      swo = mk_srd(A->wo, WO_BYTES);
      WAIT_VM(9);
      STAMP(1);
#pragma unroll 1
      for (int k = 0; k < 7; ++k) {
        if (k == 0 || !(ABL & 4)) bar_raw(); LD(ldX(1); ldW();) halo(0); adv(0); WAIT_VM(9);
        BK1(); LD(ldW(); ldX(0);) halo(1); adv(1); WAIT_VM(9);
      }
      BK1(); LD(ldX(1); ldW();) halo(0); adv(0); WAIT_VM(9);
      BK1(); LD(ldW();) halo(1); adv(1); WAIT_VM(6);
      BK1(); load_wo(swo, lb, lane, cg, hd); halo(0); adv(0); WAIT_VM(9);
      BK1(); halo(1);
    } else {
      char* ws = A->ws;
      swo = mk_srd(A->wo, WO_BYTES);
      tag = ld_flag(ws + WS_EPOCH);
      const u32 wM = rowb + ((off0 + 72 * (p >> 1) + 32) ^ s0) + ((p & 1) ? WV : WK);
      u32x4 bm = a;
#pragma unroll 1
      for (int k = 0; k < 9; ++k) {
        if (k == 0 || !(ABL & 4)) bar_raw();
        if (k == 8) load_wo(swo, lb, lane, cg, hd);
        hm = mma(bm, a, hm); halo(0); bm = lds_tr2(wM + wr, wM + wr + 768); adv(0);
        BK1();
        hm = mma(bm, a, hm); halo(1); bm = lds_tr2(wM + wr, wM + wr + 768); adv(1);
      }
      hm = mma(bm, a, hm);
    }
    hk = mma(bk, a, hk); hv = mma(a, bv, hv);
    lds_w64(lb + L_EX + hd * 1024 + lane * 8, pk4(hk));
    lds_w64(lb + L_EX + hd * 1024 + 512 + lane * 8, pk4(hv));
    if (!LOADER) {
      LAS u32* e = (LAS u32*)(lb + E_HX + (g >> 1) * 256 + u * 16 + (g & 1) * 4);
      e[0] = pk(hm[0], hm[1]); e[2] = pk(hm[2], hm[3]);
    }
    STAMP(2);
    load_wo(swo, lb, lane, cg, hd + 4);
    STAMP(3);
  }
  asm volatile("" : "+v"(tag));
  return tag + 1;
}

DEV void b_main(DG argp A, u32 lb, int xcd, int slot, int w, int lane) {
  const int i = 4 * (xcd >> 1) + (slot >> 2), j = 4 * (xcd & 1) + (slot & 3);
  const int g = lane >> 4, q = (lane & 15) >> 2, p = lane & 3, u = lane & 15;
  const int hd = w & 3;
  const u32 off0 = (144 * j) & 63, rowb = lb + L_W + (8 * g + q) * 192, s0 = (g & 1) << 5;
  const u32 wA = rowb + ((off0 + 72 * (hd >> 1) + 40 * (hd & 1) + 8 * p) ^ s0);
  const u32 xr = lb + L_X + u * 128 + ((g ^ (u >> 1)) << 4);
  char* ws = A->ws;
  const int pi = w >> 2;
  {
    const int n = slot + 16 * w;
    if (n < NB) touch_unit(lb + L_SINK, n, xcd, mk_srd(A->x, ACT_BYTES), mk_srd(A->wq, W_BYTES), lane * 1152, lane < 60 ? (lane / 5) * 1152 + (lane % 5) * 128 : 0x80000000u);
  }
  const rsrc_t rws = mk_rsrc(ws, WS_END);
  u32 oadr = WS_OM + ((i * 8 + pi * 4 + (lane >> 5)) * 16 + 2 * j + (hd >> 1)) * 1024 + (hd & 1) * 512 + (lane & 31) * 16;
  u32 xadr = WS_OX + ((i * 2 + pi) * 32 + j * 4 + hd) * 256 + lane * 4;
  lds_w128(lb + E_MX + (w * 64 + lane) * 16, u32x4{0, 0, 0, 0});
  if (w == 0 && lane < 16) lds_w128(lb + E_MX + 8192 + lane * 16, u32x4{0, 0, 0, 0});
  u32 mC[2] = {0, 0}, mP[2] = {0, 0};
#pragma unroll
  for (int r = 0; r < 4; ++r) {
    const int t = 4 * g + r;
    mC[r >> 1] |= (t <= u && t + 7 >= u) ? 0xffffu << (16 * (r & 1)) : 0u;
    mP[r >> 1] |= (t >= u + 9) ? 0xffffu << (16 * (r & 1)) : 0u;
  }
  const u32 eb = lb + E_MX + (pi * 2 + (hd >> 1)) * 4 * ESTR, hb = lb + E_HX + (hd >> 1) * 256 + 8 * (hd & 1);
  u32 aW = eb + (hd & 1) * 2 * ESTR + u * 16 + (g == 0 ? 0 : g == 1 ? 256 : 260);
  u32 aQ = g == 0 ? eb + u * 16 + 8 * (hd & 1) : eb + 512;
  u32 aK = eb + 256 + u * 16 + 8 * (hd & 1);
  u32 aF = eb + p * ESTR + 256 + (4 * g + q) * 16 + 8 * (hd & 1);
  u32 aE = p ? aF - ESTR : hb + (8 * pi + 4 * (g & 1) + q) * 16;
  u32 aHK = lb + L_EX + hd * 1024 + (g * 16 + (u & 7) + 8 * pi) * 8;
  u32 aHX = hb + (8 * pi + (u & 7)) * 16;
  u32 aHV = lb + L_EX + hd * 1024 + 512 + (((g & 1) + 2 * pi) * 16 + u) * 8;
  asm volatile("" : "+v"(mC[0]), "+v"(mC[1]), "+v"(mP[0]), "+v"(mP[1]), "+v"(aW), "+v"(aQ), "+v"(aK), "+v"(aF), "+v"(aE), "+v"(aHK), "+v"(aHX), "+v"(aHV), "+v"(oadr), "+v"(xadr));
  const float nb1 = i == 0 ? -__builtin_inff() : 0.f;
  f32x4 nb = {nb1, nb1, nb1, nb1};
  u32 oadr2 = oadr + 32768;
  asm volatile("s_waitcnt lgkmcnt(0)" : "+v"(nb), "+v"(oadr2) :: "memory");
  u64 lp[4] = {lm_prev(0), lm_prev(1), lm_prev(2), lm_prev(3)}, ll[4] = {lm_low(0), lm_low(1), lm_low(2), lm_low(3)};
  u64 le = 0x0000ffff0000ffffull, lh = 0xffffffffull;
  asm volatile("" : "+s"(lp[0]), "+s"(lp[1]), "+s"(lp[2]), "+s"(lp[3]), "+s"(ll[0]), "+s"(ll[1]), "+s"(ll[2]), "+s"(ll[3]), "+s"(le), "+s"(lh) :: "vcc");
  f32x4 acc[3][4], mac[2];
  {
    const u32 wM = rowb + ((off0 + 72 * (hd >> 1) + 32) ^ s0) + (p == 0 ? 0 : p == 1 ? WK : WV);
    if (hd & 1) gemm1<1>(DA acc, mac, wA, wM, xr + pi * 8192, rws, oadr, oadr2, xadr); else gemm1<0>(DA acc, mac, wA, wM, xr + pi * 8192, rws, oadr, oadr2, xadr);
  }
  STAMP(2);
#pragma unroll
  for (int n = 0; n < 2; ++n) { LAS u32* e = (LAS u32*)(aW + n * ESTR); e[0] = pk(mac[n][0], mac[n][1]); e[2] = pk(mac[n][2], mac[n][3]); }
  SB();
  u32x2 P[3][4];
#pragma unroll
  for (int c = 0; c < 3; ++c)
#pragma unroll
    for (int t = 0; t < 4; ++t) P[c][t] = pk4(acc[c][t]);
  SB();
  WAIT_VM(0);
  bar();
  STAMP(9);

  const f32x4 Z = {0, 0, 0, 0};
  u32x4 Q[4], K[5];
#pragma unroll
  for (int t = 0; t < 4; ++t) {
    const u32x2 qx = lds_r64v(aQ + t * ESTR), kx = lds_r64v(aK + t * ESTR);
    Q[t] = u32x4{P[0][t].x, P[0][t].y, qx.x, qx.y}; K[t + 1] = u32x4{P[1][t].x, P[1][t].y, kx.x, kx.y};
  }
  { const u32x2 a = lds_r64(aHK), b = lds_r64(aHX); K[0] = u32x4{a.x, a.y, b.x, b.y}; }
  const u32x2 vh = lds_r64(aHV);
  const u32x4 VX = lds_tr2(aE, aF);
  SB();
  f32x4 sc[4], sp[4];
#pragma unroll
  for (int t = 0; t < 4; ++t) { sc[t] = mma(K[t + 1], Q[t], Z); if (t) sp[t] = mma(K[t], Q[t], Z); }
  sp[0] = mma(K[0], Q[0], nb);
  float X[4], Y[4];
#pragma unroll
  for (int r = 0; r < 4; ++r) {
    float a = lself(lp[r], sp[2][r], sc[2][r]), b = lself(lp[r], sp[3][r], sc[3][r]);
    swap32f(a, b);
    Y[r] = lself(ll[r], a, b);
    a = lself(lp[r], sp[0][r], sc[0][r]); b = lself(lp[r], sp[1][r], sc[1][r]);
    swap32f(a, b);
    X[r] = lself(ll[r], a, b);
    swap16f(X[r], Y[r]);
  }
  const float cs = 0.23570226039551584f * 1.4426950408889634f;
  const float mx = fmx3(fmx3(X[0], X[1], X[2]), fmx3(X[3], Y[0], Y[1]), fmx3(Y[2], Y[3], Y[3]));
  const float nm = -mx * cs;
#pragma unroll
  for (int r = 0; r < 4; ++r) { X[r] = __builtin_amdgcn_exp2f(__builtin_fmaf(X[r], cs, nm)); Y[r] = __builtin_amdgcn_exp2f(__builtin_fmaf(Y[r], cs, nm)); }
  f32x2 e0 = {X[0], X[1]}, e1 = {X[2], X[3]}, e2 = {Y[0], Y[1]}, e3 = {Y[2], Y[3]};
  const f32x2 es = (e0 + e1) + (e2 + e3);
  const float rs = __builtin_amdgcn_rcpf(es.x + es.y);
  e0 *= rs; e1 *= rs; e2 *= rs; e3 *= rs;
  u32 G[4][2];
#pragma unroll
  for (int e = 0; e < 2; ++e) {
    u32 a = e ? pk(e1.x, e1.y) : pk(e0.x, e0.y), b = e ? pk(e3.x, e3.y) : pk(e2.x, e2.y);
    swap16(a, b);
    u32 a2 = a, b2 = b;
    swap32(a, a2); swap32(b, b2);
    G[0][e] = a; G[1][e] = a2; G[2][e] = b; G[3][e] = b2;
  }
  f32x4 o[4], ox[4];
#pragma unroll
  for (int t = 0; t < 4; ++t) {
    const u32x4 Pm = {G[t][0] & mP[0], G[t][1] & mP[1], G[t][0] & mC[0], G[t][1] & mC[1]};
    const u32x2 vp = t ? P[2][t - 1] : vh;
    o[t] = mma(u32x4{vp.x, vp.y, P[2][t].x, P[2][t].y}, Pm, Z);
    ox[t] = mma(VX, Pm, Z);
  }
#pragma unroll
  for (int t = 0; t < 4; t += 2) {
    u32 a0 = pk(o[t][0], o[t][1]), a1 = pk(o[t][2], o[t][3]), b0 = pk(o[t + 1][0], o[t + 1][1]), b1 = pk(o[t + 1][2], o[t + 1][3]);
    swap32(a0, b0); swap32(a1, b1);
    __builtin_amdgcn_raw_buffer_store_b128(u32x4{a0, a1, b0, b1}, rws, t ? oadr2 : oadr, 0, 16);
  }
  {
    const u32 lo = lsel(le, pk(ox[0][2], ox[0][3]), pk(ox[1][2], ox[1][3]));
    const u32 hi = lsel(le, pk(ox[2][2], ox[2][3]), pk(ox[3][2], ox[3][3]));
    __builtin_amdgcn_raw_buffer_store_b32(lsel(lh, lo, hi), rws, xadr, 0, 16);
  }
  STAMP(3);
}

DEV u32 kv_adr(int a, int oc, int lane) { return (lane >> 3) * 32768 + (((lane & 7) ^ ((oc >> 1) & 7)) << 4) + WS_KVX + a * 262144 + oc * 128; }
DEV void p0_issue(argp A, u32 lb, int a, int blk, int w, int lane) {
  const u32x4 sk_ = mk_srd(A->k, ACT_BYTES), sv = mk_srd(A->v, ACT_BYTES);
  const int s0 = 256 * blk + 32 * w, r8 = lane >> 3;
  const u32 st = lb + C_P0 + w * 9216;
  const u32 vo = r8 * 1152 + (((lane & 7) ^ (((r8 >> 1) & 1) << 2)) << 4);
  const u32 so = s0 * 1152 + a * 128;
  dma16(sk_, lane < 32 ? 0x80000000u : vo, s0 ? so - 8 * 1152 : so, st);
  dma16i<0, 1024>(sk_, vo, so, st); dma16i<0, 5120>(sv, vo, so, st); dma16i<9216, 2048>(sk_, vo, so, st); dma16i<9216, 6144>(sv, vo, so, st);
  dma16i<18432, 3072>(sk_, vo, so, st); dma16i<18432, 7168>(sv, vo, so, st); dma16i<27648, 4096>(sk_, vo, so, st); dma16i<27648, 8192>(sv, vo, so, st);
}
DEV void p0_finish(char* ws, u32x2 wv, u32 lb, int a, int blk, int w, int lane) {
  const int s0 = 256 * blk + 32 * w;
  const u32 st = lb + C_P0 + w * 9216;
  const u32 w10 = __builtin_amdgcn_alignbit(wv.x, wv.x, 16), w32 = __builtin_amdgcn_alignbit(wv.y, wv.y, 16);
  const int g = lane >> 4, q = (lane & 15) >> 2, p = lane & 3;
  const u32 ra = st + q * 128 + ((32 * g + 8 * p) ^ ((q >> 1) << 6));
  u32 Kd[18], Vd[16];
#pragma unroll
  for (int n = 0; n < 9; ++n) { u32x2 t = lds_tr(ra + 512 + n * 512); Kd[2 * n] = t.x; Kd[2 * n + 1] = t.y; }
#pragma unroll
  for (int n = 0; n < 8; ++n) { u32x2 t = lds_tr(ra + 5120 + n * 512); Vd[2 * n] = t.x; Vd[2 * n + 1] = t.y; }
  if (s0 == 0) { Kd[0] = 0; Kd[1] = 0; }
  const rsrc_t rws = mk_rsrc(ws, WS_END);
  u32 ko = __builtin_amdgcn_alignbit(Kd[1], Kd[0], 16);
#pragma unroll
  for (int n = 0; n < 4; ++n) {
    u32x4 o;
#pragma unroll
    for (int e = 0; e < 4; ++e) {
      const int m = 4 * n + e;
      const u32 kn = __builtin_amdgcn_alignbit(Kd[m + 2], Kd[m + 1], 16);
      const float ev = dot2(kn, w10, dot2(ko, w32, 0.f)) * blo(Vd[m]);
      const float od = dot2(Kd[m + 2], w10, dot2(Kd[m + 1], w32, 0.f)) * bhi(Vd[m]);
      ko = kn;
      o[3 - e] = pk(od, ev);
    }
    const int oc = 32 * blk + 4 * w + n;
    __builtin_amdgcn_raw_buffer_store_b128(o, rws, (ABL & 16) ? 0x80000000u + o.x % 7 : kv_adr(a, oc, lane), 0, 16);
  }
}

template <bool EXTRA>
DEV void conv_main(DG argp A, const u32x4 (&H)[4], const u32 (&dv)[2], u64 tag, u32 lb, int a, int sub, int w, int lane, char* flag) {
  const int g = lane >> 4, r = lane & 15;
  const int ch0 = a * 64 + sub * 8;
  char* ws = A->ws;
  const int blk = EXTRA ? (w & 3) : w, set = EXTRA ? (w >> 2) : 0;
  constexpr u32 OOFF = EXTRA ? 2112 : 4160;
  const u32 hb = lb + C_H + w * HSTR;
  if (lane < (EXTRA ? 8 : 4)) lds_w128(hb + (lane >> 2) * 4192 + ((lane >> 1) & 1) * OOFF + (lane & 1) * 16, u32x4{0, 0, 0, 0});
  u32 pv[4];
#pragma unroll
  for (int n = 0; n < 4; ++n) lds_w128(hb + (EXTRA ? (n >> 1) * 4192 + (n & 1) * 1024 : n * 1024) + 32 + lane * 16, H[n]);
#pragma unroll
  for (int n = 0; n < 4; ++n) pv[n] = lds_r32(hb + (EXTRA ? (n >> 1) * 4192 + (n & 1) * 1024 : n * 1024) + 28 + lane * 16);
#pragma unroll
  for (int n = 0; n < 4; ++n) {
    const u32x4 o = {__builtin_amdgcn_alignbit(H[n].x, pv[n], 16), __builtin_amdgcn_alignbit(H[n].y, H[n].x, 16), __builtin_amdgcn_alignbit(H[n].z, H[n].y, 16), __builtin_amdgcn_alignbit(H[n].w, H[n].z, 16)};
    lds_w128(hb + OOFF + (EXTRA ? (n >> 1) * 4192 + (n & 1) * 1024 : n * 1024) + 32 + lane * 16, o);
  }
  SB();
  WAIT_VM(0);
  if (flag && lane == 0) st_flag(flag, tag);
  asm volatile("" :: "v"(tag));
  STAMP(12);
  bar_raw();
  lds_w64(lb + C_X + (w * 64 + lane) * 8, u32x2{0, 0});
  if (EXTRA) lds_w64(lb + C_X + 20480 + (w * 64 + lane) * 8, u32x2{0, 0});
  const u32 aa = hb + ((r & 1) ? 0 : OOFF) + ((r >> 1) + 1 + 4 * g) * 4;
  auto baddr = [&](int b) -> u32 { const int o = 33 + 2 * r - g - 4 * b; return lb + C_X + o * 128 + ((w ^ ((o >> 1) & 7)) << 4); };
  auto boff = [](int c) -> u32 { return EXTRA ? (c >> 2) * 20480 + (c & 3) * 4096 : c * 4096; };
  auto aoff = [](int c) -> u32 { return EXTRA ? (c >> 2) * 4192 + (c & 3) * 512 : c * 512; };
  constexpr int NA = EXTRA ? 8 : 4;
  f32x4 acc[NA];
  u32x2 Al[8], Ah[8];
  u32x4 B[8];
  u32 ab[4], bb4[4];
#pragma unroll
  for (int n = 0; n < 4; ++n) { ab[n] = aa + aoff(2 * n); bb4[n] = baddr(4 + n); asm volatile("" : "+v"(ab[n]), "+v"(bb4[n])); }
  auto ld = [&](int c, int part, int b) {
    if (b > 7) return;
    const LAS u32* pa = (const LAS u32*)(ab[c >> 1] + (c & 1) * 512 + b * 64);
    const int cb = EXTRA ? c ^ 3 : 7 - c;
    if (part == 0) Al[c] = u32x2{pa[0], pa[1]};
    else if (part == 1) Ah[c] = u32x2{pa[2], pa[3]};
    else B[cb] = lds_r128(bb4[b & 3] + (b < 4 ? 2048 : 0) + boff(cb));
  };
  u32x4 AD[2];
  {
    const int jd = 15 - r - 8 * g;
#pragma unroll
    for (int n = 0; n < 2; ++n) {
      const u32 d = dv[n] << (16 * (jd & 1));
      AD[n] = u32x4{(jd >> 1) == 0 ? d : 0u, (jd >> 1) == 1 ? d : 0u, (jd >> 1) == 2 ? d : 0u, (jd >> 1) == 3 ? d : 0u};
    }
    asm volatile("" : "+v"(AD[0]));
    if (EXTRA) asm volatile("" : "+v"(AD[1]));
  }
#pragma unroll
  for (int c = 0; c < 6; ++c) { ld(c, 0, 0); ld(c, 1, 0); }
  SB();
  const rsrc_t rws = mk_rsrc(ws + WS_KVX + (a * 8 + sub + set) * 32768 + blk * 4096, 4096);
  bar_raw();
  STAMP(2);
  u32x4 xv[4];
#pragma unroll
  for (int n = 0; n < 4; ++n) xv[n] = __builtin_amdgcn_raw_buffer_load_b128(rws, lane * 16 + n * 1024, 0, 16);
#pragma unroll
  for (int n = 0; n < 4; ++n) lds_w128(lb + C_X + set * 20480 + 4096 + blk * 4096 + n * 1024 + lane * 16, xv[n]);
  bar();
  STAMP(4);
#pragma unroll
  for (int c = 0; c < 8; ++c) { ld(c, 2, 0); SB(); }
#pragma unroll
  for (int c = 0; c < NA; ++c) { const int t = EXTRA ? c ^ 3 : 3 - c; acc[t] = mma(AD[EXTRA ? t >> 2 : 0], B[EXTRA ? t : 4 + t], f32x4{0, 0, 0, 0}); SB(); }
#pragma unroll
  for (int b = 0; b < 8; ++b) {
    int slot_ = 0;
#pragma unroll
    for (int c = 0; c < 8; ++c) {
      const int thi = EXTRA ? (c | 3) : 7, tlo = EXTRA ? c : (c > 4 ? c : 4);
#pragma unroll
      for (int t = thi; t >= tlo; --t) {
        const u32x4 Av = {Al[c].x, Al[c].y, Ah[c].x, Ah[c].y};
        if (EXTRA) acc[t] = mma(Av, B[(c & 4) + t - c], acc[t]); else acc[t - 4] = mma(Av, B[t - c], acc[t - 4]);
        SB();
        const int n = slot_++;
        if (t == thi) ld(c, 2, b + 1);
        if (!EXTRA) {
          if (n == 1 || n == 2) ld(6, n - 1, b);
          else if (n == 3) { if (b) ld(5, 1, b); }
          else if (n == 7 || n == 11) ld(7, n >> 3, b);
          else if (n == 24) ld(5, 0, b + 1);
          else if (n < 23 && (n & 3) && (n & 3) < 3) ld((n >> 2) - 1, (n & 3) - 1, b + 1);
        } else {
          if (n == 1) { ld(6, 0, b); ld(6, 1, b); }
          else if (n == 2 || n == 3) ld(7, n - 2, b);
          else if (n == 5 || n == 6) ld(0, n - 5, b + 1);
          else if (n == 8) { ld(1, 0, b + 1); ld(1, 1, b + 1); }
          else if (n == 11) { ld(2, 0, b + 1); ld(2, 1, b + 1); }
          else if (n == 12 || n == 13) ld(3, n - 12, b + 1);
          else if (n == 15 || n == 16) ld(4, n - 15, b + 1);
          else if (n == 18) { ld(5, 0, b + 1); ld(5, 1, b + 1); }
        }
        SB();
      }
    }
  }
  STAMP(5);
#pragma unroll
  for (int t = 0; t < NA; ++t) lds_w64(hb + t * 512 + r * 32 + g * 8, pk4(acc[t]));
  bar();
  STAMP(6);
  const rsrc_t ro = mk_rsrc(A->out, OUT_BYTES);
  const int q = r >> 2, p = lane & 3;
  u64 odd = 0xaaaaaaaaaaaaaaaaull;
  asm volatile("" : "+s"(odd) :: "vcc");
#pragma unroll
  for (int n = 0; n < 2; ++n) {
    const int row = w * 128 + n * 64;
    const u32 za = lb + C_H + q * HSTR + (row + 16 * g) * 2 + 8 * p;
    u32x4 z[2];
    z[0] = lds_tr2(za, za + 4 * HSTR);
    if (EXTRA) {
      z[1] = lds_tr2(za + 2048, za + 2048 + 4 * HSTR);
#pragma unroll
      for (int e = 0; e < 4; ++e) {
        const u32 t0 = z[0][e], t1 = z[1][e];
        z[0][e] = __builtin_amdgcn_update_dpp(t0, t1, 0xa0, 0xf, 0xf, false);
        z[1][e] = __builtin_amdgcn_update_dpp(t1, t0, 0xf5, 0xf, 0xf, false);
        z[0][e] = lsel(odd, z[0][e], t0); z[1][e] = lsel(odd, t1, z[1][e]);
      }
    }
#pragma unroll
    for (int s = 0; s < (EXTRA ? 2 : 1); ++s) {
      const int rr = EXTRA ? row + (lane & ~1) + s : row + lane, cc = EXTRA ? (lane & 1) * 16 : 0;
      const u32x4 qq = lds_r128(lb + C_Q + (EXTRA ? rr * 32 + cc : rr * 16));
      u32x4 o;
#pragma unroll
      for (int e = 0; e < 4; ++e) o[e] = pk(blo(z[s][e]) * blo(qq[e]), bhi(z[s][e]) * bhi(qq[e]));
      __builtin_amdgcn_raw_buffer_store_b128(o, ro, ((EXTRA ? 0 : 1024) + rr) * 2304 + ch0 * 2 + cc, 0, AUXA);
    }
  }
}

DEV int late_role(int xcd, int slot, int& ca, int& sub) {
  const bool ninth = slot == 24 || slot == 25;
  ca = ninth ? 8 : xcd;
  if (slot < 24) { sub = slot - 16; return 0; }
  if (ninth) { sub = xcd < 4 ? 2 * xcd + slot - 24 : 4 * (xcd - 4) + 2 * (slot - 24); return xcd < 4 ? 0 : xcd < 6 ? 1 : 2; }
  sub = 2 * (slot - 26);
  return slot < 30 ? 1 : 3;
}

DEV void nb_main(DG argp A, u32 lb, int xcd, int slot, int w, int lane) {
  const bool p0 = slot <= 24;
  char* ws = A->ws;
  u32x2 wv = {0, 0};
  u64 tag;
  if (p0) {
    p0_issue(A, lb, slot - 16, xcd, w, lane);
    wv = *(const u32x2*)(A->sk + ((slot - 16) * 64 + lane) * 4);
  }
  tag = ld_flag(ws + WS_EPOCH);
  int ca, sub;
  const int late = late_role(xcd, slot, ca, sub);
  const int ch = late == 2 ? 0 : ca * 64 + sub * 8 + w;
  const u16* hp = A->h + ch * 2048 + lane * 8;
  const u16 *d0 = A->Dv + ch, *d1 = A->Dv + (late == 1 ? ch + 8 : ch);
  if (p0) {
    WAIT_VM(0);
    STAMP(1);
  }
  u32x4 H[4];
  u32 dv[2];
#pragma unroll
  for (int n = 0; n < 4; ++n) H[n] = *(const u32x4*)(hp + (n & 1) * 512 + (n >> 1) * (late == 1 ? 8 * 2048 : 1024));
  dv[0] = *d0; dv[1] = *d1;
  SB();
  if (p0) {
    const int n = NB + 7 * NE + 9 * (w - (8 - NP)) + slot - 16;
    if (w >= 8 - NP && n < 225) touch_unit(lb + L_SINK, n, xcd, mk_srd(A->x, ACT_BYTES), mk_srd(A->wq, W_BYTES), lane * 1152, lane < 60 ? (lane / 5) * 1152 + (lane % 5) * 128 : 0x80000000u);
    p0_finish(ws, wv, lb, slot - 16, xcd, w, lane);
    STAMP(9);
  }
  tag += 1;
  char* flag = p0 ? ws + WS_KVF + ((slot - 16) * 8 + xcd) * 128 + w * 8 : nullptr;
  if (late == 0) conv_main<false>(DA A, H, dv, tag, lb, ca, sub, w, lane, flag);
  else if (late == 1) conv_main<true>(DA A, H, dv, tag, lb, ca, sub, w, lane, flag);
  else { WAIT_VM(0); if (flag && lane == 0) st_flag(flag, tag); asm volatile("" :: "v"(tag)); bar_raw(); }
  FIN();
}

DEV u64 nb_aux(DG argp A, u32 lb, int xcd, int slot, int i2, int w, int lane) {
  const u32 sink = lb + L_SINK;
  u64 tag = 0;
  if (w == 9 && slot > 24) {
    const u32x4 sx = mk_srd(A->x, ACT_BYTES), sw = mk_srd(A->wq, W_BYTES);
    const u32 vx = lane * 1152, vw = lane < 60 ? (lane / 5) * 1152 + (lane % 5) * 128 : 0x80000000u;
#pragma unroll 1
    for (int n = NB + slot - 25; n < NB + 7 * NE; n += 7) { touch_unit(sink, n, xcd, sx, sw, vx, vw); if (PACE && n >= NB + 7 * NFAST) __builtin_amdgcn_s_sleep(PACE); }
    if (slot >= 30) {
#pragma unroll 1
      for (int n = NB + 7 * NE + 9 * NP + slot - 30; n < NLATE; n += 2) touch_unit(sink, n, xcd, sx, sw, vx, vw);
    }
    STAMP(1);
  } else if (w == 8) {
    tag = ld_flag(A->ws + WS_EPOCH) + 1;
    asm volatile("" :: "v"(tag));
  }
  int ca, sub;
  const int late = late_role(xcd, slot, ca, sub);
  if (late < 2) { if (w == 9) WAIT_VM(PFW); bar_raw(); }
  if (w == 8 && late < 2) poll<KGAP>(A->ws + WS_KVF + (ca * 8 + ((late ? lane & 31 : lane) >> 3)) * 128 + (lane & 7) * 8, tag);
  bar_raw();
  if (w == 8 && lane == 0) st_flag(A->ws + WS_CHECK + blockIdx.x * 8, tag);
  if (late == 3) {
    const u32x4 swo = mk_srd(A->wo, WO_BYTES);
    load_wo(swo, lb, lane, slot - 23, w - 8); load_wo(swo, lb, lane, slot - 23, w - 4);
    return tag;
  }
  if (late < 2) {
    bar_raw();
    if (slot != 24 && slot != 25) {
      const u32x4 so = mk_srd(A->wo, WO_BYTES), sx = mk_srd(A->x, ACT_BYTES), sw = mk_srd(A->wq, W_BYTES);
      const u32 vx = lane * 1152, vw = lane < 60 ? (lane / 5) * 1152 + (lane % 5) * 128 : 0x80000000u;
#pragma unroll 1
      for (int n = NLATE + (slot < 24 ? slot - 16 : slot - 18) + 12 * (w - 8); n < 225 + 81; n += 48) {
        if (n < 225) touch_unit(sink, n, xcd, sx, sw, vx, vw); else touch(sink, so, lane * 128, (n - 225) * 8192);
      }
    }
    const u32x4 sq = mk_srd(A->q, ACT_BYTES);
    const int ch0 = ca * 64 + sub * 8;
    const u32 vq = late ? (lane >> 1) * 1152 + (lane & 1) * 16 : lane * 1152;
#pragma unroll 1
    for (int n = w - 8; n < (late ? 32 : 16); n += 4) {
      const int bl = late ? (n + 4 * sub) & 31 : (n + 2 * sub) & 15;
      dma16(sq, vq, (late ? bl * 32 : 1024 + bl * 64) * 1152 + ch0 * 2, lb + C_Q + bl * 1024);
    }
    WAIT_VM(0);
    bar_raw();
  } else if (slot == 25 && xcd == 7 && w == 8) {
#pragma unroll 1
    for (int n = 0; n < 4; ++n) poll<8>(A->ws + WS_CHECK + (n * 64 + lane) * 8, tag);
    if (lane == 0) st_flag(A->ws + WS_EPOCH, tag);
  }
  WAIT_VM(0);
  FIN();
  return 0;
}

#define STR2(x) #x
#define STR(x) STR2(x)
extern "C" __global__ __launch_bounds__(NT) void fused_kernel(Args) {
  __shared__ __attribute__((aligned(16))) char lds[LDS_BYTES];
  asm volatile(
      "s_load_dword s95, %0, 0x0\n\ts_load_dword s95, %0, 0x40\n\t"
      "v_readfirstlane_b32 s3, %2\n\t"
      "s_cmpk_lt_u32 s3, 0x200\n\ts_cbranch_scc1 1f\n\t"
      "s_cmpk_lt_u32 %1, 0x80\n\ts_cbranch_scc0 2f\n\t"
      "s_cmpk_lt_u32 s3, 0x240\n\ts_cbranch_scc0 r_load\n\ts_branch r_halo\n"
      "1:\n\ts_cmpk_lt_u32 %1, 0x80\n\ts_cbranch_scc1 r_bmain\n\t"
      "s_cmpk_lt_u32 %1, 0xf0\n\ts_cbranch_scc1 r_nmain\n\ts_branch r_bmain\n"
      "2:\n\ts_cmpk_lg_u32 s3, 0x280\n\ts_cbranch_scc1 r_naux\n\t"
      "s_lshr_b32 s3, %1, 3\n\ts_sub_u32 s3, s3, 25\n\ts_cmp_ge_u32 s3, " STR(CODE_UNITS) "\n\ts_cbranch_scc1 r_naux\n\t"
      "s_getpc_b64 s[4:5]\n\ts_and_b32 s4, s4, 0xffffff00\n\ts_lshl_b32 s3, s3, 6\n\t"
      "v_and_b32 v167, 63, %2\n\tv_add_lshl_u32 v167, v167, s3, 7\n\tv_min_u32 v167, r_end-fused_kernel, v167\n\tglobal_load_dword v167, v167, s[4:5]\n\ts_branch r_naux"
      :: "s"(__builtin_amdgcn_kernarg_segment_ptr()), "s"(blockIdx.x), "v"(threadIdx.x), "s"((u32)(size_t)(LAS char*)lds)
      : "s3", "s4", "s5", "s90", "s91", "s92", "s93", "s95", "v167", "scc", "vcc", "memory");
}
#define ROLE extern "C" __global__ __launch_bounds__(NT) void
#define ENTER()
#define IDS() ENTER(); const argp A = (argp)__builtin_amdgcn_kernarg_segment_ptr(); constexpr u32 lb = 0; \
  const int xcd = blockIdx.x & 7, slot = blockIdx.x >> 3, w = __builtin_amdgcn_readfirstlane(threadIdx.x >> 6), lane = threadIdx.x & 63; \
  const int i2 = 2 * xcd + (slot >= 9), cg = slot - (slot >= 9) * 9 - (slot >= 16) * 14;
template <int KIND>
DEV void aux_tail(DG argp A, u32 lb, int lane, int w, int i2, u64 tag) {
  const Relay R = relay_adr(lb, lane, w - 8);
  const char* cp = A->ws + WS_CHECK + (((i2 & 3) * 4 + (lane & 3)) * 8 + 2 * (i2 >> 2) + ((lane >> 2) & 1)) * 8;
  if (KIND < 2) { bar(); __builtin_amdgcn_s_setprio(RPRIO); }
  if (KIND == 1) { if (lane == 0) st_flag(A->ws + WS_CHECK + blockIdx.x * 8, tag); WAIT_VM(10); } else WAIT_VM(9);
  relay_wo(R, 0);
  WAIT_VM(0);
  u64 f = tag;
  if (KIND == 1) f = ld_flag(cp);
  SB();
  relay_wo(R, 4 * 9216);
  SB();
  if (KIND == 1 ? !__all(f == tag) : KIND == 2 && w == 8) poll<KIND == 1 ? 1 : 8>(cp, tag);
  STAMP(4);
  gemm2_aux(DA A->ws, lb, lane, w, i2, KIND == 2);
  FIN();
}
ROLE r_load(Args) { IDS(); b_aux<true>(DA A, lb, xcd, slot, cg, i2, w, lane); aux_tail<0>(DA A, lb, lane, w, i2, 0); }
ROLE r_naux(Args) { IDS(); const u64 tag = nb_aux(DA A, lb, xcd, slot, i2, w, lane); aux_tail<2>(DA A, lb, lane, w, i2, tag); }
ROLE r_halo(Args) { IDS(); const u64 tag = b_aux<false>(DA A, lb, xcd, slot, cg, i2, w, lane); aux_tail<1>(DA A, lb, lane, w, i2, tag); }
ROLE r_bmain(Args) { IDS(); if (__builtin_expect(slot < 16, 1)) b_main(DA A, lb, xcd, slot, w, lane); else bar_raw(); gemm2(DA A->ws, A->out, lb, lane, w, i2, cg); FIN(); }
ROLE r_nmain(Args) { IDS(); nb_main(DA A, lb, xcd, slot, w, lane); }
#define FITS(k) ".if (" #k ".private_seg_size | " #k ".num_agpr | " #k ".has_dyn_sized_stack | (" #k ".num_vgpr > 168) | (" #k ".numbered_sgpr > 88)) != 0\n\t.error \"role exceeds budget\"\n\t.endif\n\t"
ROLE r_end(Args) { asm volatile(FITS(r_load) FITS(r_naux) FITS(r_halo) FITS(r_bmain) FITS(r_nmain) ".if r_naux.num_vgpr > 160\n\t.error \"role exceeds budget\"\n\t.endif"); }

extern "C" void kernel_launch(void* const* d_in, const int* in_sizes, int n_in, void* d_out, int out_size, void* d_ws, size_t ws_size,
                              hipStream_t stream) {
  const Args a = {(const u16*)d_in[0], (const u16*)d_in[1], (const u16*)d_in[2], (const u16*)d_in[3], (const u16*)d_in[4], (const u16*)d_in[5],
                  (const u16*)d_in[6], (const u16*)d_in[7], (const u16*)d_in[8], (u16*)d_out, (char*)d_ws};
  fused_kernel<<<256, NT, 0, stream>>>(a);
}
```

```cpp
#define AUXA 18
#define AUXB 18
#include <hip/hip_runtime.h>
#include <hip/hip_bf16.h>
#pragma clang diagnostic ignored "-Wint-to-pointer-cast"

typedef unsigned u32;
typedef unsigned short u16;
typedef unsigned long long u64;
typedef __attribute__((ext_vector_type(4))) u32 u32x4;
typedef __attribute__((ext_vector_type(2))) u32 u32x2;
typedef __attribute__((ext_vector_type(4))) float f32x4;
typedef __attribute__((ext_vector_type(2))) float f32x2;
typedef __attribute__((ext_vector_type(8))) __bf16 bf16x8;
typedef __attribute__((ext_vector_type(2))) __bf16 bf16x2;
typedef __attribute__((ext_vector_type(4))) short s16x4;
#define LAS __attribute__((address_space(3)))
#define GAS __attribute__((address_space(1)))
#define DEV __device__ __forceinline__
typedef __amdgpu_buffer_rsrc_t rsrc_t;

constexpr int NT = 768, NWV = 12;
constexpr u32 ACT_BYTES = 2048u * 1152u;
constexpr u32 W_BYTES = 576u * 3456u;
constexpr u32 WO_BYTES = 576u * 1152u;
constexpr u32 OUT_BYTES = 2048u * 2304u;
constexpr u32 WS_EPOCH = 0, WS_CHECK = 4096, WS_KVF = 8192, WS_OF = 16384;
constexpr u32 WS_KVX = 65536, WS_OM = WS_KVX + 2359296, WS_OX = WS_OM + 2097152, WS_END = WS_OX + 262144;
constexpr u32 WS_DIAG = 8u << 20;
constexpr u32 SLOT = 18432;
constexpr u32 L_W = 0, L_X = 5 * SLOT, L_WO = 3 * SLOT, L_EX = 8 * SLOT;
constexpr u32 L_TAG = L_EX + 29 * 512;
constexpr u32 L_SINK = L_TAG + 16;
constexpr u32 LDS_BYTES = L_SINK + 256;
constexpr u32 HSTR = 8384;
constexpr u32 C_H = 0, C_X = 8 * HSTR, C_Q = C_X + 40960, C_P0 = C_X;
static_assert(C_P0 + 8 * 9216 <= L_TAG && C_Q + 32768 <= L_TAG, "lds");
constexpr u64 SPIN_LIMIT = 400000;
#define CODE_BYTES 8192

#define STAMP(n) do {} while (0)
#define DG
#define DA

DEV u32 pk(float a, float b) { f32x2 v = {a, b}; return __builtin_bit_cast(u32, __builtin_convertvector(v, bf16x2)); }
DEV float blo(u32 x) { return __uint_as_float(x << 16); }
DEV float bhi(u32 x) { return __uint_as_float(x & 0xffff0000u); }
DEV u32x2 lds_tr(u32 a) { return __builtin_bit_cast(u32x2, __builtin_amdgcn_ds_read_tr16_b64_v4i16((LAS s16x4*)a)); }
DEV u32x4 lds_tr2(u32 a, u32 b) { u32x2 x = lds_tr(a), y = lds_tr(b); return u32x4{x.x, x.y, y.x, y.y}; }
DEV u32x4 lds_r128(u32 a) { return *(const LAS u32x4*)a; }
DEV u32x2 lds_r64(u32 a) { return *(const LAS u32x2*)a; }
DEV u32 lds_r32(u32 a) { return *(const LAS u32*)a; }
DEV u32x4 lds_r4x32(u32 a) { const LAS u32* p = (const LAS u32*)a; return u32x4{p[0], p[1], p[2], p[3]}; }
DEV void lds_w128(u32 a, u32x4 v) { *(LAS u32x4*)a = v; }
DEV void lds_w64(u32 a, u32x2 v) { *(LAS u32x2*)a = v; }
DEV f32x4 mma(u32x4 a, u32x4 b, f32x4 c) { return __builtin_amdgcn_mfma_f32_16x16x32_bf16(__builtin_bit_cast(bf16x8, a), __builtin_bit_cast(bf16x8, b), c, 0, 0, 0); }
DEV f32x4 mma16(u32x2 a, u32x2 b, f32x4 c) { return __builtin_amdgcn_mfma_f32_16x16x16bf16_1k(__builtin_bit_cast(s16x4, a), __builtin_bit_cast(s16x4, b), c, 0, 0, 0); }
DEV float dot2(u32 a, u32 b, float c) { return __builtin_amdgcn_fdot2_f32_bf16(__builtin_bit_cast(bf16x2, a), __builtin_bit_cast(bf16x2, b), c, false); }
DEV u32x2 pk4(f32x4 v) { return u32x2{pk(v[0], v[1]), pk(v[2], v[3])}; }
DEV void swap16(u32& a, u32& b) { auto r = __builtin_amdgcn_permlane16_swap(a, b, false, false); a = r[0]; b = r[1]; }
DEV void swap32(u32& a, u32& b) { auto r = __builtin_amdgcn_permlane32_swap(a, b, false, false); a = r[0]; b = r[1]; }
DEV void swap16f(float& a, float& b) { u32 x = __float_as_uint(a), y = __float_as_uint(b); swap16(x, y); a = __uint_as_float(x); b = __uint_as_float(y); }
DEV void swap32f(float& a, float& b) { u32 x = __float_as_uint(a), y = __float_as_uint(b); swap32(x, y); a = __uint_as_float(x); b = __uint_as_float(y); }
DEV float fmx(float a, float b) { float r; asm("v_max_f32 %0, %1, %2" : "=v"(r) : "v"(a), "v"(b)); return r; }
DEV void bar() { asm volatile("s_waitcnt lgkmcnt(0)\n\ts_barrier" ::: "memory"); }
DEV void bar_raw() { asm volatile("s_barrier" ::: "memory"); }
#define SB() __builtin_amdgcn_sched_barrier(0)
#define WAIT_VM(n) asm volatile("s_waitcnt vmcnt(" #n ")" ::: "memory")

DEV u32x4 mk_srd(const void* p, u32 bytes) { u64 a = (u64)p; return u32x4{(u32)a, (u32)(a >> 32) & 0xffffu, bytes, 0x20000u}; }
DEV rsrc_t mk_rsrc(const void* p, u32 bytes) { return __builtin_amdgcn_make_buffer_rsrc((void*)p, 0, bytes, 0x20000); }
DEV void dma16(u32x4 srd, u32 voff, u32 soff, u32 lds) {
  asm volatile("s_mov_b32 m0, %3\n\ts_nop 0\n\tbuffer_load_dwordx4 %0, %1, %2 offen lds" :: "v"(voff), "s"(srd), "s"(soff), "s"(lds) : "memory");
}
DEV void touch(u32 sink, u32x4 srd, u32 voff, u32 soff) {
  asm volatile("s_mov_b32 m0, %3\n\ts_nop 0\n\tbuffer_load_dword %0, %1, %2 offen lds" :: "v"(voff), "s"(srd), "s"(soff), "s"(sink) : "memory");
}
DEV u64 ld_flag(const char* p) { return __hip_atomic_load((const GAS u64*)p, __ATOMIC_RELAXED, __HIP_MEMORY_SCOPE_AGENT); }
DEV void st_flag(char* p, u64 v) { __hip_atomic_store((GAS u64*)p, v, __ATOMIC_RELAXED, __HIP_MEMORY_SCOPE_AGENT); }
DEV void poll(const char* p, u64 tag) {
  u64 t0 = __builtin_amdgcn_s_memrealtime();
  for (;;) {
    u64 v = ld_flag(p);
    if (__all(v == tag)) break;
    if (__builtin_amdgcn_s_memrealtime() - t0 > SPIN_LIMIT) break;
    __builtin_amdgcn_s_sleep(1);
  }
  asm volatile("" ::: "memory");
}
DEV u64 read_tag(u32 lb) { u32x2 t = lds_r64(lb + L_TAG); return ((u64)t.y << 32) | t.x; }
DEV void epoch_in(char* ws, u32 lb, int lane) {
  u64 tag = ld_flag(ws + WS_EPOCH) + 1;
  if (lane == 0) { lds_w64(lb + L_TAG, u32x2{(u32)tag, (u32)(tag >> 32)}); st_flag(ws + WS_CHECK + blockIdx.x * 8, tag); }
}
DEV void touch_unit(u32 sink, int n, int xcd, u32x4 sx, u32x4 sw, u32 vx, u32 vw) {
  int kc = n / 25, r = n - kc * 25;
  if (r < 9) {
    int row0 = 512 * (xcd >> 1) + (r < 8 ? 64 * r : -64);
    if (row0 >= 0) touch(sink, sx, r < 8 ? vx : (vx < 48 * 1152 ? 0x80000000u : vx), row0 * 1152 + kc * 128);
  } else {
    touch(sink, sw, vw, (kc * 192 + (r - 9) * 12) * 1152 + (xcd & 1) * 512);
  }
}

template <int HSEL>
DEV void gemm1(DG f32x4 (&acc)[2][7], f32x4& hacc, u32 lb, int lane, int pi, int half, int pair, int j) {
  const int g = lane >> 4, q = (lane & 15) >> 2, p = lane & 3, i = lane & 15;
  const u32 off0 = (144 * j) & 63;
  const u32 rowb = lb + L_W + (8 * g + q) * 576;
  const u32 s0 = (g & 1) << 5, s1 = s0 | 16;
  const u32 cA = off0 + 72 * pair + 8 * p, cB = cA + 40, cM = off0 + 72 * pair + 32;
  const u32 wA0 = rowb + (cA ^ s0), wB0 = rowb + (cB ^ s0), wA1 = rowb + 192 + (cA ^ s1), wB1 = rowb + 192 + (cB ^ s1);
  const u32 wM = rowb + (p == 0 ? (cM ^ s0) : p == 1 ? 192 + (cM ^ s1) : 384 + (cM ^ s0));
  const u32 xr = lb + L_X + i * 128 + ((g ^ (i >> 1)) << 4);
  const u32 x0 = xr + (pi * 4 + half * 2) * 2048, xh = xr + 8 * 2048;
  u32x4 w[7], xa[3], xb[3];
  auto rdW = [&](int c, u32 so) -> u32x4 {
    u32 a = c == 0 ? wA0 : c == 1 ? wB0 : c == 2 ? wA1 : c == 3 ? wB1 : c == 4 ? wA0 + 384 : c == 5 ? wB0 + 384 : wM;
    return lds_tr2(a + so, a + so + 4 * 576);
  };
  auto phase = [&](u32x4 (&xc)[3], u32x4 (&xn)[3], u32 xo, u32 xx, u32 wo_) {
    xn[0] = lds_r128((x0 + xo) ^ xx); xn[1] = lds_r128(((x0 + xo) ^ xx) + 2048); xn[2] = lds_r128((xh + xo) ^ xx);
    SB();
#pragma unroll
    for (int c = 0; c < 7; ++c) {
      const bool nat = (c == 4 || c == 5);
      acc[0][c] = nat ? mma(xc[0], w[c], acc[0][c]) : mma(w[c], xc[0], acc[0][c]);
      acc[1][c] = nat ? mma(xc[1], w[c], acc[1][c]) : mma(w[c], xc[1], acc[1][c]);
      if (c == 2 + HSEL) hacc = nat ? mma(xc[2], w[c], hacc) : mma(w[c], xc[2], hacc);
      w[c] = rdW(c, wo_);
      SB();
    }
  };
  bar_raw();
  STAMP(1);
  xa[0] = lds_r128(x0); xa[1] = lds_r128(x0 + 2048); xa[2] = lds_r128(xh);
#pragma unroll
  for (int c = 0; c < 7; ++c) w[c] = rdW(c, 0);
  SB();
  u32 ws_ = 0, xs = 0;
#pragma unroll 1
  for (int k = 0; k < 9; ++k) {
    ws_ = ws_ == 4 * SLOT ? 0 : ws_ + SLOT;
    phase(xa, xb, xs, 64, ws_);
    asm volatile("" :: "v"(xb[0]), "v"(xb[1]), "v"(xb[2]));
    if (k < 8) bar_raw();
    ws_ = ws_ == 4 * SLOT ? 0 : ws_ + SLOT;
    xs = xs == 2 * SLOT ? 0 : xs + SLOT;
    phase(xb, xa, xs, 0, ws_);
  }
}

DEV void load_wo(u32x4 swo, u32 lb, int lane, int cg, int lw) {
  const int r = lane >> 3;
  const u32 vo = r * 1152 + (((lane & 7) ^ (((r >> 1) & 3) << 1)) << 4) + cg * 128 + lw * 9216;
  int src = 72 * (cg & 7);
#pragma unroll 1
  for (int n = 0; n < 72; n += 9) {
#pragma unroll
    for (int m = 0; m < 9; m += 3) dma16(swo, vo, (src + 8 * m) * 1152, lb + L_WO + (n + m + lw) * 1024);
    src = src == 504 ? 0 : src + 72;
  }
}

DEV void gemm2(DG char* ws, u16* out, u32 lb, int lane, int w, int i2, int cg, u64 tag) {
  const int g = lane >> 4, q = (lane & 15) >> 2, p = lane & 3, u = lane & 15;
  const rsrc_t rws = mk_rsrc(ws, WS_END);
  u32 bw[4], bm[4];
#pragma unroll
  for (int ct = 0; ct < 4; ++ct) {
    bw[ct] = lb + L_WO + (4 * g + q) * 128 + ((ct ^ (((4 * g + q) >> 1) & 3)) << 5) + 8 * p;
    bm[ct] = lb + L_WO + (72 * g + 16 + q) * 128 + ((ct ^ (q >> 1)) << 5) + 8 * p;
  }
  auto rdB = [&](int ks, int ct) -> u32x4 {
    if (ks < 16) { const int e = (ks & 1) << 1; return lds_tr2(bw[ct ^ e] + ks * 4608, bw[ct ^ e ^ 2] + ks * 4608 + 2560); }
    return lds_tr2(bm[ct] + (ks - 16) * 36864, bm[ct ^ 2] + (ks - 16) * 36864 + 4608);
  };
  u32x4 B[2][4];
#pragma unroll
  for (int ct = 0; ct < 4; ++ct) { B[0][ct] = rdB(0, ct); B[1][ct] = rdB(1, ct); }
  poll(ws + WS_OF + ((i2 * 4 + (w >> 1)) * 16 + (lane & 15)) * 8, tag);
  STAMP(6);
  u32x4 A[18];
  const u32 om = WS_OM + (i2 * 8 + w) * 16384;
#pragma unroll
  for (int n = 0; n < 16; ++n) A[n] = __builtin_amdgcn_raw_buffer_load_b128(rws, lane * 16, om + ((n + 2 * cg) & 15) * 1024, 16);
#pragma unroll
  for (int m = 0; m < 2; ++m) {
    const u32 vo = ((4 * m + g + cg) & 7) * 256 + u * 8;
    u32x2 a = __builtin_amdgcn_raw_buffer_load_b64(rws, vo, WS_OX + (i2 * 8 + w) * 2048, 16);
    u32x2 b = __builtin_amdgcn_raw_buffer_load_b64(rws, vo + 128, WS_OX + (i2 * 8 + w) * 2048, 16);
    A[16 + m] = u32x4{a.x, a.y, b.x, b.y};
  }
  SB();
  f32x4 acc[4] = {{0, 0, 0, 0}, {0, 0, 0, 0}, {0, 0, 0, 0}, {0, 0, 0, 0}};
#pragma unroll
  for (int ks = 0; ks < 18; ++ks) {
#pragma unroll
    for (int ct = 0; ct < 4; ++ct) {
      acc[ct] = mma(B[ks & 1][ct], A[ks], acc[ct]);
      if (ks + 2 < 18) B[ks & 1][ct] = rdB(ks + 2, ct);
    }
    SB();
  }
  STAMP(7);
  const u32 st = lb + L_W + w * 2304;
#pragma unroll
  for (int ct = 0; ct < 4; ++ct) lds_w64(st + u * 144 + ct * 32 + g * 8, pk4(acc[ct]));
  const rsrc_t ro = mk_rsrc(out, OUT_BYTES);
#pragma unroll
  for (int n = 0; n < 2; ++n) {
    const int r = 8 * n + (lane >> 3);
    u32x4 v = lds_r128(st + r * 144 + (lane & 7) * 16);
    const int row = 128 * i2 + 32 * (w & 3) + 2 * r + (w >> 2);
    __builtin_amdgcn_raw_buffer_store_b128(v, ro, row * 2304 + (lane & 7) * 16 + 1152 + cg * 128, 0, AUXB);
  }
}

DEV void role_b(DG const u16* x, const u16* wq, const u16* wo, char* ws, u32 lb, int xcd, int slot, int w, int lane) {
  const int i = 4 * (xcd >> 1) + (slot >> 2), j = 4 * (xcd & 1) + (slot & 3);
  const int cg = slot >= 9 ? slot - 9 : slot;
  const u32x4 sx = mk_srd(x, ACT_BYTES), sw = mk_srd(wq, W_BYTES);
  const int g = lane >> 4, q = (lane & 15) >> 2, p = lane & 3, u = lane & 15;
  if (w >= 9) {
    const int lw = w - 9;
    u32 vw[3];
#pragma unroll
    for (int n = 0; n < 3; ++n) {
      const int c = 64 * (3 * n + lw) + lane, row = c / 36, rem = c - row * 36, seg = rem / 12, ch = rem - seg * 12;
      vw[n] = row * 3456 + seg * 1152 + ((ch ^ ((((row >> 3) & 1) << 1) | (seg == 1))) << 4);
    }
    const int r8 = lane >> 3;
    const u32 vx0 = r8 * 2304 + (((lane & 7) ^ (r8 >> 1)) << 4), vx1 = vx0 ^ 64;
    u32 xo[3], xd[3];
#pragma unroll
    for (int n = 0; n < 3; ++n) {
      const int t = 3 * n + lw;
      xo[n] = t < 8 ? (128 * i + (t & 3) * 32 + (t >> 2)) * 1152 : (i == 0 ? 0 : (128 * i - 16) * 1152);
      xd[n] = xo[n] + (t < 8 ? 16 * 1152 : 1152);
    }
    const u32 wbase = (144 * j) & ~63;
    auto ldX = [&](int k, u32 xs) {
#pragma unroll
      for (int n = 0; n < 3; ++n) {
        dma16(sx, vx0, xo[n] + k * 128, lb + L_X + xs + (3 * n + lw) * 2048);
        dma16(sx, vx1, xd[n] + k * 128, lb + L_X + xs + (3 * n + lw) * 2048 + 1024);
      }
    };
    auto ldW = [&](int s, u32 wsl) {
#pragma unroll
      for (int hh = 0; hh < 2; ++hh)
#pragma unroll
        for (int n = 0; n < 3; ++n) dma16(sw, vw[n], (s * 32 + hh * 16) * 3456 + wbase, lb + L_W + wsl + (hh * 9 + 3 * n + lw) * 1024);
    };
    ldX(0, 0); ldW(0, 0); ldW(1, SLOT); ldX(1, SLOT);
    u32 wl = 2 * SLOT, xl = 2 * SLOT;
    WAIT_VM(6);
    STAMP(1);
#pragma unroll 1
    for (int k = 0; k < 8; ++k) {
      bar_raw();
      ldW(2 * k + 2, wl); wl = wl == 4 * SLOT ? 0 : wl + SLOT;
      ldW(2 * k + 3, wl); wl = wl == 4 * SLOT ? 0 : wl + SLOT;
      if (k < 7) { ldX(k + 2, xl); xl = xl == 2 * SLOT ? 0 : xl + SLOT; WAIT_VM(6); } else WAIT_VM(0);
    }
    bar_raw();
    STAMP(2);
    load_wo(mk_srd(wo, WO_BYTES), lb, lane, cg, lw);
    STAMP(3);
    bar_raw();
    WAIT_VM(0);
    bar_raw();
    return;
  }
  if (w == 8) {
    epoch_in(ws, lb, lane);
    const u32 off0 = (144 * j) & 63, s0 = (g & 1) << 5;
    const u32 cM = off0 + 72 * (p >> 1) + 32;
    const u32 wM = lb + L_W + (8 * g + q) * 576 + ((p & 1) ? 384 + (cM ^ s0) : 192 + (cM ^ (s0 | 16)));
    const u32 xh = lb + L_X + 16384 + u * 128 + ((g ^ (u >> 1)) << 4);
    f32x4 hacc = {0, 0, 0, 0};
    u32 wr = 0, xr = 0;
#pragma unroll 1
    for (int k = 0; k < 9; ++k) {
      bar_raw();
      u32x4 a0 = lds_r128(xh + xr), a1 = lds_r128((xh + xr) ^ 64);
      u32x4 b0 = lds_tr2(wM + wr, wM + wr + 2304);
      wr = wr == 4 * SLOT ? 0 : wr + SLOT;
      u32x4 b1 = lds_tr2(wM + wr, wM + wr + 2304);
      wr = wr == 4 * SLOT ? 0 : wr + SLOT;
      xr = xr == 2 * SLOT ? 0 : xr + SLOT;
      hacc = mma(b0, a0, hacc); hacc = mma(b1, a1, hacc);
      SB();
    }
    lds_w64(lb + L_EX + 8 * 512 + lane * 8, pk4(hacc));
    bar();
    bar_raw();
    return;
  }
  const int pi = w & 1, half = (w >> 1) & 1, pair = w >> 2;
  if (w < 2) {
    const u32 sink = lb + L_SINK;
    const int n = slot + 16 * w;
    if (n < 25) touch_unit(sink, n, xcd, sx, sw, lane * 1152, lane < 60 ? (lane / 5) * 1152 + (lane % 5) * 128 : 0x80000000u);
  }
  f32x4 acc[2][7], hacc = {0, 0, 0, 0};
#pragma unroll
  for (int c = 0; c < 7; ++c) { acc[0][c] = f32x4{0, 0, 0, 0}; acc[1][c] = f32x4{0, 0, 0, 0}; }
  switch (w & 3) {
    case 0: gemm1<0>(DA acc, hacc, lb, lane, pi, half, pair, j); break;
    case 1: gemm1<1>(DA acc, hacc, lb, lane, pi, half, pair, j); break;
    case 2: gemm1<2>(DA acc, hacc, lb, lane, pi, half, pair, j); break;
    default: gemm1<3>(DA acc, hacc, lb, lane, pi, half, pair, j); break;
  }
  STAMP(2);
  u32x2 P[2][7];
#pragma unroll
  for (int t = 0; t < 2; ++t)
#pragma unroll
    for (int c = 0; c < 7; ++c) P[t][c] = pk4(acc[t][c]);
  lds_w64(lb + L_EX + w * 512 + lane * 8, pk4(hacc));
  const u32 pkb = lb + L_EX + (9 + (pair * 2 + pi) * 5) * 512;
  if (half == 0) {
#pragma unroll
    for (int c = 2; c < 7; ++c) lds_w64(pkb + (c - 2) * 512 + lane * 8, P[1][c]);
  }
  bar();
  STAMP(9);
  const u64 tag = read_tag(lb);
  u32x2 V[5];
  {
    const u32 hk = lb + L_EX + pair * 2048 + (g * 16 + (u & 7) + 8 * pi) * 8;
    const u32 hv = lb + L_EX + pair * 2048 + 1024 + (((g & 1) + 2 * pi) * 16 + u) * 8;
    const u32 hm = lb + L_EX + 8 * 512 + ((2 * pair + (g >> 1)) * 16 + (u & 7) + 8 * pi) * 8;
    const u32 ak = half ? pkb + lane * 8 : hk, av = half ? pkb + 1024 + lane * 8 : hv, am = half ? pkb + 2048 + lane * 8 : hm;
    V[0] = lds_r64(ak); V[1] = lds_r64(ak + 512); V[2] = lds_r64(av); V[3] = lds_r64(av + 512); V[4] = lds_r64(am);
  }
  const float NEG = -__builtin_inff();
  f32x4 bC, bP, bP0;
  u32 mC[2] = {0, 0}, mP[2] = {0, 0};
  const bool first = (i == 0 && half == 0);
#pragma unroll
  for (int r = 0; r < 4; ++r) {
    const int t = 4 * g + r;
    const bool vc = t <= u && t + 7 >= u, vp = t >= u + 9;
    bC[r] = vc ? 0.f : NEG; bP[r] = vp ? 0.f : NEG; bP0[r] = (vp && !first) ? 0.f : NEG;
    mC[r >> 1] |= vc ? 0xffffu << (16 * (r & 1)) : 0u;
    mP[r >> 1] |= vp ? 0xffffu << (16 * (r & 1)) : 0u;
  }
  const u32x2 sel = {g == 3 ? (u == 0 ? 0x3f80u : u == 1 ? 0x3f800000u : 0u) : 0u, g == 3 ? (u == 2 ? 0x3f80u : u == 3 ? 0x3f800000u : 0u) : 0u};
  const f32x4 Z = {0, 0, 0, 0};
  u32x2 VX[3] = {pk4(mma16(V[4], sel, Z)), pk4(mma16(P[0][6], sel, Z)), pk4(mma16(P[1][6], sel, Z))};
  float M[4][4];
#pragma unroll
  for (int t = 0; t < 2; ++t)
#pragma unroll
    for (int hd = 0; hd < 2; ++hd) {
      u32 a = 0, m = P[t][6][hd];
      swap16(a, m);
      a = g == 1 ? a : 0u;
      const u32x4 Q = {P[t][hd].x, P[t][hd].y, a, 0u};
      const u32x4 Kc = {P[t][2 + hd].x, P[t][2 + hd].y, P[t][6][hd], 0u};
      const u32x2 kp = t ? P[0][2 + hd] : V[hd];
      const u32x4 Kp = {kp.x, kp.y, t ? P[0][6][hd] : V[4][hd], 0u};
      const f32x4 sc = mma(Kc, Q, bC), sp = mma(Kp, Q, t ? bP : bP0);
#pragma unroll
      for (int r = 0; r < 4; ++r) M[t * 2 + hd][r] = __builtin_fmaxf(sc[r], sp[r]);
    }
  float X[4], Y[4];
#pragma unroll
  for (int r = 0; r < 4; ++r) {
    swap32f(M[0][r], M[1][r]); swap32f(M[2][r], M[3][r]);
    X[r] = __builtin_fmaxf(M[0][r], M[1][r]); Y[r] = __builtin_fmaxf(M[2][r], M[3][r]);
    swap16f(X[r], Y[r]);
  }
  const float cs = 0.23570226039551584f * 1.4426950408889634f;
  const float mx = __builtin_fmaxf(__builtin_fmaxf(__builtin_fmaxf(X[0], X[1]), __builtin_fmaxf(X[2], X[3])), __builtin_fmaxf(__builtin_fmaxf(Y[0], Y[1]), __builtin_fmaxf(Y[2], Y[3])));
  const float nm = -mx * cs;
  float sum = 0.f;
#pragma unroll
  for (int r = 0; r < 4; ++r) { X[r] = __builtin_amdgcn_exp2f(__builtin_fmaf(X[r], cs, nm)); Y[r] = __builtin_amdgcn_exp2f(__builtin_fmaf(Y[r], cs, nm)); sum += X[r] + Y[r]; }
  const float rs = __builtin_amdgcn_rcpf(sum);
  u32 G[4][2];
#pragma unroll
  for (int e = 0; e < 2; ++e) {
    u32 a = pk(X[2 * e] * rs, X[2 * e + 1] * rs), b = pk(Y[2 * e] * rs, Y[2 * e + 1] * rs);
    swap16(a, b);
    u32 a2 = a, b2 = b;
    swap32(a, a2); swap32(b, b2);
    G[0][e] = a; G[1][e] = a2; G[2][e] = b; G[3][e] = b2;
  }
  const rsrc_t rws = mk_rsrc(ws, WS_END);
#pragma unroll
  for (int t = 0; t < 2; ++t) {
    f32x4 o[2], ox[2];
#pragma unroll
    for (int hd = 0; hd < 2; ++hd) {
      const u32* gg = G[t * 2 + hd];
      const u32x4 Pm = {gg[0] & mP[0], gg[1] & mP[1], gg[0] & mC[0], gg[1] & mC[1]};
      const u32x2 vp = t ? P[0][4 + hd] : V[2 + hd];
      o[hd] = mma(u32x4{vp.x, vp.y, P[t][4 + hd].x, P[t][4 + hd].y}, Pm, Z);
      ox[hd] = mma(u32x4{VX[t].x, VX[t].y, VX[t + 1].x, VX[t + 1].y}, Pm, Z);
    }
    const int rt = pi * 4 + half * 2 + t;
    const u32x2 oa = pk4(o[0]), ob = pk4(o[1]);
    __builtin_amdgcn_raw_buffer_store_b128(u32x4{oa.x, oa.y, ob.x, ob.y}, rws, lane * 16 + WS_OM + ((i * 8 + rt) * 16 + 2 * j + pair) * 1024, 0, 16);
    if (g == 0) __builtin_amdgcn_raw_buffer_store_b64(u32x2{pk(ox[0][0], ox[0][1]), pk(ox[1][2], ox[1][3])}, rws, u * 8 + WS_OX + ((i * 8 + rt) * 16 + 2 * j + pair) * 128, 0, 16);
  }
  STAMP(3);
  WAIT_VM(0);
  if (lane == 0) st_flag(ws + WS_OF + ((i * 4 + pi * 2 + half) * 16 + 2 * j + pair) * 8, tag);
  STAMP(4);
  bar_raw();
  STAMP(5);
}

DEV void role_p0(DG const u16* k, const u16* v, const u16* sk, char* ws, u32 lb, int a, int blk, int w, int lane) {
  const u32x4 sk_ = mk_srd(k, ACT_BYTES), sv = mk_srd(v, ACT_BYTES);
  const int s0 = 256 * blk + 32 * w, r8 = lane >> 3;
  const u32 st = lb + C_P0 + w * 9216;
  const u32 vo = r8 * 1152 + (((lane & 7) ^ (((r8 >> 1) & 1) << 2)) << 4);
  const u32 so = s0 * 1152 + a * 128;
  if (lane >= 32) dma16(sk_, vo, s0 ? so - 8 * 1152 : so, st);
#pragma unroll
  for (int n = 0; n < 4; ++n) { dma16(sk_, vo, so + n * 9216, st + 1024 + n * 1024); dma16(sv, vo, so + n * 9216, st + 5120 + n * 1024); }
  const u32x2 wv = *(const u32x2*)(sk + (a * 64 + lane) * 4);
  const u32 w10 = __builtin_amdgcn_alignbit(wv.x, wv.x, 16), w32 = __builtin_amdgcn_alignbit(wv.y, wv.y, 16);
  WAIT_VM(0);
  STAMP(1);
  const int g = lane >> 4, q = (lane & 15) >> 2, p = lane & 3;
  const u32 ra = st + q * 128 + ((32 * g + 8 * p) ^ ((q >> 1) << 6));
  u32 Kd[18], Vd[16];
#pragma unroll
  for (int n = 0; n < 9; ++n) { u32x2 t = lds_tr(ra + 512 + n * 512); Kd[2 * n] = t.x; Kd[2 * n + 1] = t.y; }
#pragma unroll
  for (int n = 0; n < 8; ++n) { u32x2 t = lds_tr(ra + 5120 + n * 512); Vd[2 * n] = t.x; Vd[2 * n + 1] = t.y; }
  if (s0 == 0) { Kd[0] = 0; Kd[1] = 0; }
  const rsrc_t rws = mk_rsrc(ws, WS_END);
  u32 ko = __builtin_amdgcn_alignbit(Kd[1], Kd[0], 16);
#pragma unroll
  for (int n = 0; n < 4; ++n) {
    u32x4 o;
#pragma unroll
    for (int e = 0; e < 4; ++e) {
      const int m = 4 * n + e;
      const u32 kn = __builtin_amdgcn_alignbit(Kd[m + 2], Kd[m + 1], 16);
      const float ev = dot2(kn, w10, dot2(ko, w32, 0.f)) * blo(Vd[m]);
      const float od = dot2(Kd[m + 2], w10, dot2(Kd[m + 1], w32, 0.f)) * bhi(Vd[m]);
      ko = kn;
      o[3 - e] = pk(od, ev);
    }
    const int oc = 32 * blk + 4 * w + n;
    __builtin_amdgcn_raw_buffer_store_b128(o, rws, r8 * 32768 + (((lane & 7) ^ ((oc >> 1) & 7)) << 4) + WS_KVX + a * 262144 + oc * 128, 0, 16);
  }
}

template <bool EXTRA>
DEV void role_conv(DG const u16* qp, const u16* h, const u16* Dv, u16* out, char* ws, u32 lb, int a, int sub, int w, int lane, bool didp0, int pa, int pblk) {
  const int g = lane >> 4, r = lane & 15;
  const int ch0 = a * 64 + sub * 8;
  if (w >= 8) {
    bar();
    bar_raw();
    const u32x4 sq = mk_srd(qp, ACT_BYTES);
    const int rot = EXTRA ? 2 * sub : 2 * sub;
#pragma unroll 1
    for (int n = w - 8; n < 16; n += 4) {
      const int bl = (n + rot) & 15;
      if (EXTRA) {
        dma16(sq, (lane >> 1) * 1152 + (lane & 1) * 16, (bl * 64) * 1152 + ch0 * 2, lb + C_Q + bl * 2048);
        dma16(sq, (lane >> 1) * 1152 + (lane & 1) * 16, (bl * 64 + 32) * 1152 + ch0 * 2, lb + C_Q + bl * 2048 + 1024);
      } else {
        dma16(sq, lane * 1152, (1024 + bl * 64) * 1152 + ch0 * 2, lb + C_Q + bl * 1024);
      }
    }
    WAIT_VM(0);
    bar_raw();
    return;
  }
  STAMP(9);
  u32x4 H[4];
#pragma unroll
  for (int n = 0; n < 4; ++n) {
    const int ch = EXTRA ? ch0 + 8 * (n >> 1) + w : ch0 + w, e0 = EXTRA ? (n & 1) * 512 : n * 512;
    H[n] = *(const u32x4*)(h + ch * 2048 + e0 + lane * 8);
  }
  u32 dv[2];
  dv[0] = Dv[ch0 + w]; dv[1] = EXTRA ? Dv[ch0 + 8 + w] : 0;
  bar_raw();
  const u64 tag = read_tag(lb);
  if (didp0) {
    WAIT_VM(0);
    if (lane == 0) st_flag(ws + WS_KVF + ((pa * 8 + pblk) * 8 + w) * 8, tag);
  }
  STAMP(2);
  constexpr u32 OOFF = EXTRA ? 2112 : 4160;
  const u32 hb = lb + C_H + w * HSTR;
  if (lane < (EXTRA ? 8 : 4)) lds_w128(hb + (lane >> 2) * 4192 + ((lane >> 1) & 1) * OOFF + (lane & 1) * 16, u32x4{0, 0, 0, 0});
  lds_w64(lb + C_X + (w * 64 + lane) * 8, u32x2{0, 0});
  if (EXTRA) lds_w64(lb + C_X + 20480 + (w * 64 + lane) * 8, u32x2{0, 0});
  u32 pv[4];
#pragma unroll
  for (int n = 0; n < 4; ++n) lds_w128(hb + (EXTRA ? (n >> 1) * 4192 + (n & 1) * 1024 : n * 1024) + 32 + lane * 16, H[n]);
#pragma unroll
  for (int n = 0; n < 4; ++n) pv[n] = lds_r32(hb + (EXTRA ? (n >> 1) * 4192 + (n & 1) * 1024 : n * 1024) + 28 + lane * 16);
#pragma unroll
  for (int n = 0; n < 4; ++n) {
    const u32x4 o = {__builtin_amdgcn_alignbit(H[n].x, pv[n], 16), __builtin_amdgcn_alignbit(H[n].y, H[n].x, 16), __builtin_amdgcn_alignbit(H[n].z, H[n].y, 16), __builtin_amdgcn_alignbit(H[n].w, H[n].z, 16)};
    lds_w128(hb + OOFF + (EXTRA ? (n >> 1) * 4192 + (n & 1) * 1024 : n * 1024) + 32 + lane * 16, o);
  }
  const int blk = EXTRA ? (w & 3) : w, set = EXTRA ? (w >> 2) : 0;
  poll(ws + WS_KVF + ((a * 8 + blk) * 8 + (lane & 7)) * 8, tag);
  STAMP(3);
  const rsrc_t rws = mk_rsrc(ws, WS_END);
  u32x4 xv[4];
#pragma unroll
  for (int n = 0; n < 4; ++n) xv[n] = __builtin_amdgcn_raw_buffer_load_b128(rws, lane * 16 + n * 1024, WS_KVX + (a * 8 + sub + set) * 32768 + blk * 4096, 16);
#pragma unroll
  for (int n = 0; n < 4; ++n) lds_w128(lb + C_X + set * 20480 + 4096 + blk * 4096 + n * 1024 + lane * 16, xv[n]);
  bar();
  STAMP(4);
  const u32 aa = hb + ((r & 1) ? 0 : OOFF) + ((r >> 1) + 1 + 4 * g) * 4;
  auto baddr = [&](int b) -> u32 { const int o = 33 + 2 * r - g - 4 * b; return lb + C_X + o * 128 + ((w ^ ((o >> 1) & 7)) << 4); };
  auto boff = [](int c) -> u32 { return EXTRA ? (c >> 2) * 20480 + (c & 3) * 4096 : c * 4096; };
  auto aoff = [](int c) -> u32 { return EXTRA ? (c >> 2) * 4192 + (c & 3) * 512 : c * 512; };
  constexpr int NA = EXTRA ? 8 : 4;
  f32x4 acc[NA];
  {
    const int jd = 15 - r - 8 * g;
    const u32 b0 = baddr(0);
#pragma unroll
    for (int t = 0; t < NA; ++t) {
      const u32 d = dv[EXTRA ? t >> 2 : 0] << (16 * (jd & 1));
      const u32x4 AD = {(jd >> 1) == 0 ? d : 0u, (jd >> 1) == 1 ? d : 0u, (jd >> 1) == 2 ? d : 0u, (jd >> 1) == 3 ? d : 0u};
      acc[t] = mma(AD, lds_r128(b0 + boff(EXTRA ? t : 4 + t)), f32x4{0, 0, 0, 0});
    }
  }
  u32x4 A[8], B[8];
  {
    const u32 ba = baddr(0);
#pragma unroll
    for (int c = 0; c < 8; ++c) { const int cb = EXTRA ? c ^ 3 : 7 - c; A[c] = lds_r4x32(aa + aoff(c)); B[cb] = lds_r128(ba + boff(cb)); }
  }
  SB();
#pragma unroll 1
  for (int b = 1; b < 9; ++b) {
    const u32 ba = baddr(b & 7), ab = aa + 64 * (b & 7);
#pragma unroll
    for (int c = 0; c < 8; ++c) {
      if (EXTRA) {
#pragma unroll
        for (int t = (c | 3); t >= c; --t) acc[t] = mma(A[c], B[(c & 4) + t - c], acc[t]);
      } else {
#pragma unroll
        for (int t = 7; t >= (c > 4 ? c : 4); --t) acc[t - 4] = mma(A[c], B[t - c], acc[t - 4]);
      }
      SB();
      const int cb = EXTRA ? c ^ 3 : 7 - c;
      A[c] = lds_r4x32(ab + aoff(c)); B[cb] = lds_r128(ba + boff(cb));
      SB();
    }
  }
  STAMP(5);
#pragma unroll
  for (int t = 0; t < NA; ++t) lds_w64(hb + t * 512 + r * 32 + g * 8, pk4(acc[t]));
  bar();
  STAMP(6);
  const rsrc_t ro = mk_rsrc(out, OUT_BYTES);
  const int q = r >> 2, p = lane & 3;
#pragma unroll
  for (int n = 0; n < 2; ++n)
#pragma unroll
    for (int s = 0; s < (EXTRA ? 2 : 1); ++s) {
      const int row = w * 128 + n * 64;
      const u32 za = lb + C_H + q * HSTR + s * 2048 + (row + 16 * g) * 2 + 8 * p;
      const u32x4 z = lds_tr2(za, za + 4 * HSTR);
      const u32x4 qq = lds_r128(lb + C_Q + (EXTRA ? (row + lane) * 32 + s * 16 : (row + lane) * 16));
      u32x4 o;
#pragma unroll
      for (int e = 0; e < 4; ++e) o[e] = pk(blo(z[e]) * blo(qq[e]), bhi(z[e]) * bhi(qq[e]));
      __builtin_amdgcn_raw_buffer_store_b128(o, ro, lane * 2304 + ((EXTRA ? 0 : 1024) + row) * 2304 + ch0 * 2 + s * 16, 0, AUXA);
    }
}

__global__ __launch_bounds__(NT) void fused_kernel(const u16* __restrict__ q, const u16* __restrict__ k, const u16* __restrict__ v,
                                                   const u16* __restrict__ sk, const u16* __restrict__ h, const u16* __restrict__ Dv,
                                                   const u16* __restrict__ x, const u16* __restrict__ wq, const u16* __restrict__ wo,
                                                   u16* __restrict__ out, char* __restrict__ ws) {
  __shared__ __attribute__((aligned(16))) char lds[LDS_BYTES];
  const u32 lb = (u32)(size_t)(LAS char*)lds;
  const int xcd = blockIdx.x & 7, slot = blockIdx.x >> 3;
  const int w = __builtin_amdgcn_readfirstlane(threadIdx.x >> 6), lane = threadIdx.x & 63;
  STAMP(0);
  if (slot < 16) {
    role_b(DA x, wq, wo, ws, lb, xcd, slot, w, lane);
  } else {
  const bool p0 = slot <= 24;
  if (w >= 8) {
    if (w == 8) epoch_in(ws, lb, lane);
  } else if (p0) {
    role_p0(DA k, v, sk, ws, lb, slot - 16, xcd, w, lane);
  } else if (w == 0) {
    const u32 sink = lb + L_SINK;
    const u32x4 sx = mk_srd(x, ACT_BYTES), sw = mk_srd(wq, W_BYTES), so = mk_srd(wo, WO_BYTES);
    const u32 vx = lane * 1152, vw = lane < 60 ? (lane / 5) * 1152 + (lane % 5) * 128 : 0x80000000u;
#pragma unroll 1
    for (int n = 25 + slot - 25; n < 225; n += 7) touch_unit(sink, n, xcd, sx, sw, vx, vw);
#pragma unroll 1
    for (int n = slot - 25; n < 81; n += 7) touch(sink, so, lane * 128, n * 8192);
    WAIT_VM(0);
    STAMP(1);
  }
  const bool ninth = slot == 24 || slot == 25;
  const int ca = ninth ? 8 : xcd;
  if (slot < 24 || (ninth && xcd < 4)) role_conv<false>(DA q, h, Dv, out, ws, lb, ca, ninth ? 2 * xcd + slot - 24 : slot - 16, w, lane, p0, slot - 16, xcd);
  else if (slot < 30 && (!ninth || xcd < 6)) role_conv<true>(DA q, h, Dv, out, ws, lb, ca, ninth ? 4 * (xcd - 4) + 2 * (slot - 24) : 2 * (slot - 26), w, lane, p0, slot - 16, xcd);
  else {
    if (slot >= 30 && w >= 9) load_wo(mk_srd(wo, WO_BYTES), lb, lane, slot - 23, w - 9);
    WAIT_VM(0);
    bar();
    const u64 tag = read_tag(lb);
    if (p0) {
      if (w < 8 && lane == 0) st_flag(ws + WS_KVF + ((8 * 8 + xcd) * 8 + w) * 8, tag);
    } else if (slot == 25 && xcd == 7 && w == 0) {
#pragma unroll 1
      for (int n = 0; n < 4; ++n) poll(ws + WS_CHECK + (n * 64 + lane) * 8, tag);
      if (lane == 0) st_flag(ws + WS_EPOCH, tag);
    }
  }
  }
  if ((slot < 16 || slot >= 30) && w < 8) gemm2(DA ws, out, lb, lane, w, 2 * xcd + (slot >= 9), slot < 9 ? slot : slot < 16 ? slot - 9 : slot - 23, read_tag(lb));
  STAMP(8);
}

extern "C" void kernel_launch(void* const* d_in, const int* in_sizes, int n_in, void* d_out, int out_size, void* d_ws, size_t ws_size,
                              hipStream_t stream) {
  fused_kernel<<<256, NT, 0, stream>>>((const u16*)d_in[0], (const u16*)d_in[1], (const u16*)d_in[2], (const u16*)d_in[3], (const u16*)d_in[4],
                                       (const u16*)d_in[5], (const u16*)d_in[6], (const u16*)d_in[7], (const u16*)d_in[8], (u16*)d_out, (char*)d_ws);
}
```

```cpp
#include <hip/hip_runtime.h>
#include <hip/hip_bf16.h>
#pragma clang diagnostic ignored "-Wint-to-pointer-cast"

typedef unsigned u32;
typedef unsigned short u16;
typedef unsigned long long u64;
typedef __attribute__((ext_vector_type(4))) u32 u32x4;
typedef __attribute__((ext_vector_type(2))) u32 u32x2;
typedef __attribute__((ext_vector_type(4))) float f32x4;
typedef __attribute__((ext_vector_type(2))) float f32x2;
typedef __attribute__((ext_vector_type(8))) __bf16 bf16x8;
typedef __attribute__((ext_vector_type(2))) __bf16 bf16x2;
typedef __attribute__((ext_vector_type(4))) short s16x4;
#define LAS __attribute__((address_space(3)))
#define GAS __attribute__((address_space(1)))
#define DEV __device__ __forceinline__
typedef __amdgpu_buffer_rsrc_t rsrc_t;

#define WKO 16
#define WVO 24
constexpr int NT = 768, NWV = 12;
constexpr u32 ACT_BYTES = 2048u * 1152u;
constexpr u32 W_BYTES = 576u * 3456u;
constexpr u32 WO_BYTES = 576u * 1152u;
constexpr u32 OUT_BYTES = 2048u * 2304u;
constexpr u32 WS_EPOCH = 0, WS_CHECK = 4096;
constexpr u32 WS_KVX = 131072, WS_OM = WS_KVX + 2359296, WS_OX = WS_OM + 2097152, WS_END = WS_OX + 262144;
constexpr u32 WS_DIAG = 8u << 20;
constexpr u32 SLOT = 18432, WSLOT = 18560, WK = 6144 + WKO, WV = 2 * 6144 + WVO;
constexpr u32 L_W = 0, L_X = 5 * WSLOT, L_WO = 3 * WSLOT, L_EX = L_X + 3 * SLOT;
constexpr u32 L_BM = 2 * WSLOT;
constexpr u32 L_OX = WSLOT;
constexpr u32 E_HX = L_EX + 4096, E_MX = E_HX + 512, ESTR = 528;
constexpr u32 L_TAG = E_MX + 16 * ESTR;
constexpr u32 L_SINK = L_TAG + 32;
constexpr u32 LDS_BYTES = L_SINK + 256;
constexpr u32 HSTR = 8384;
constexpr u32 C_H = 0, C_X = 8 * HSTR, C_Q = C_X + 40960, C_P0 = C_X;
constexpr u32 C_S = C_P0 + 8 * 9216;
static_assert(C_S + 8 * 1536 <= L_TAG && C_Q + 32768 <= L_TAG, "lds");
constexpr u32 SENT = 0xffffffffu;
constexpr int SPIN_LIMIT = 40000;
#define ABL 0
#define AUXA 0
#define AUXB 0
#define CODE_UNITS 7
#define KGAP 2
#define DGAP 6
#define G2STAMP 0
#define CLRSTEP 15
#define ROTM 15
#define ROT(c) ((c) & ROTM)
#define XDELAY 8
#define NB 25
#define NP 3
#define PFW 1
#define AAUX 16
#define PSHARE 0
#define PDELAY 4
#define EVEN 0
#define NF 3
#define BMASK 0
#define HS0 40
#define HS1 45
#define WMASK 0
#define PFQ 2
#define CWAIT 1
#define HPOS 2
#define EW 12
#define RFIRST 0
#define CPOS 0
#define XL 3
#define XS 4
#define LATEPOS 0
#define LS 6
#define OWNPOLL 1
#define VW 0
#define NG 0
#define HG 8
#define HN 4
#define HD 0
#define NPW 2
#define SW 1
#define SX0 0
#define SX1 0
#define NTKV 1
#define NTH 0
#define NTT 0
#define PB 150
#define EARLYF 1
#define SAN 1
#define OTA 0
#define OTB 0
#define GDA 6
#define GDB 4
#define F3STEP 12
#define PGAP 1
#define NH 6
#define RPRIO 0
#define PACE 0
#define NFAST 8
#define NE 23
#define CSLEEP 80
#define CLR_FIRST 1

#define STAMP(n) do {} while (0)
#define DG
#define DA

DEV u32 pk(float a, float b) { f32x2 v = {a, b}; return __builtin_bit_cast(u32, __builtin_convertvector(v, bf16x2)); }
DEV float blo(u32 x) { return __uint_as_float(x << 16); }
DEV float bhi(u32 x) { return __uint_as_float(x & 0xffff0000u); }
DEV u32x2 lds_tr(u32 a) { return __builtin_bit_cast(u32x2, __builtin_amdgcn_ds_read_tr16_b64_v4i16((LAS s16x4*)a)); }
DEV u32x4 lds_tr2(u32 a, u32 b) { u32x2 x = lds_tr(a), y = lds_tr(b); return u32x4{x.x, x.y, y.x, y.y}; }
DEV u32x4 lds_r128(u32 a) { return *(const LAS u32x4*)a; }
DEV u32x2 lds_r64(u32 a) { return *(const LAS u32x2*)a; }
DEV u32x2 lds_r64v(u32 a) { return *(const volatile LAS u32x2*)a; }
DEV u32 lds_r32(u32 a) { return *(const LAS u32*)a; }
DEV u32x4 lds_r4x32(u32 a) { const LAS u32* p = (const LAS u32*)a; return u32x4{p[0], p[1], p[2], p[3]}; }
DEV void lds_w128(u32 a, u32x4 v) { *(LAS u32x4*)a = v; }
DEV void lds_w64(u32 a, u32x2 v) { *(LAS u32x2*)a = v; }
DEV f32x4 mma(u32x4 a, u32x4 b, f32x4 c) { return __builtin_amdgcn_mfma_f32_16x16x32_bf16(__builtin_bit_cast(bf16x8, a), __builtin_bit_cast(bf16x8, b), c, 0, 0, 0); }
DEV f32x4 mma16(u32x2 a, u32x2 b, f32x4 c) { return __builtin_amdgcn_mfma_f32_16x16x16bf16_1k(__builtin_bit_cast(s16x4, a), __builtin_bit_cast(s16x4, b), c, 0, 0, 0); }
DEV float dot2(u32 a, u32 b, float c) { return __builtin_amdgcn_fdot2_f32_bf16(__builtin_bit_cast(bf16x2, a), __builtin_bit_cast(bf16x2, b), c, false); }
DEV u32x2 pk4(f32x4 v) { return u32x2{pk(v[0], v[1]), pk(v[2], v[3])}; }
DEV void swap16(u32& a, u32& b) { auto r = __builtin_amdgcn_permlane16_swap(a, b, false, false); a = r[0]; b = r[1]; }
DEV void swap32(u32& a, u32& b) { auto r = __builtin_amdgcn_permlane32_swap(a, b, false, false); a = r[0]; b = r[1]; }
DEV void swap16f(float& a, float& b) { u32 x = __float_as_uint(a), y = __float_as_uint(b); swap16(x, y); a = __uint_as_float(x); b = __uint_as_float(y); }
DEV void swap32f(float& a, float& b) { u32 x = __float_as_uint(a), y = __float_as_uint(b); swap32(x, y); a = __uint_as_float(x); b = __uint_as_float(y); }
DEV float fmx3(float a, float b, float c) { float r; asm("v_max3_f32 %0, %1, %2, %3" : "=v"(r) : "v"(a), "v"(b), "v"(c)); return r; }
DEV u32 live(u32 x) { return SAN ? min(x, SENT - 1) : x; }
DEV u32 lsel(u64 m, u32 a, u32 b) { return __builtin_amdgcn_inverse_ballot_w64(m) ? a : b; }
DEV float lself(u64 m, float a, float b) { return __builtin_amdgcn_inverse_ballot_w64(m) ? a : b; }
constexpr u64 LM_G1 = 0xffff0000ull;
constexpr u64 lm_prev(int r) { u64 m = 0; for (int l = 0; l < 64; ++l) if (4 * (l >> 4) + r >= (l & 15) + 9) m |= 1ull << l; return m; }
constexpr u64 lm_low(int r) { u64 m = 0; for (int l = 0; l < 64; ++l) { const int t = 4 * ((l >> 4) & 1) + r, u = l & 15; if (t <= u && t + 7 >= u) m |= 1ull << l; } return m; }
DEV void bar() { asm volatile("s_waitcnt lgkmcnt(0)\n\ts_barrier" ::: "memory"); }
DEV void bar_raw() { asm volatile("s_barrier" ::: "memory"); }
#define BK1() do { if (!(ABL & 4)) bar_raw(); } while (0)
#define LD(x) if (!(ABL & 1)) { x }
#define SB() __builtin_amdgcn_sched_barrier(0)
#define WAIT_VM(n) asm volatile("s_waitcnt vmcnt(%0)" :: "n"(n) : "memory")

DEV u32x4 mk_srd(const void* p, u32 bytes) { u64 a = (u64)p; return u32x4{(u32)a, (u32)(a >> 32) & 0xffffu, bytes, 0x20000u}; }
DEV rsrc_t mk_rsrc(const void* p, u32 bytes) { return __builtin_amdgcn_make_buffer_rsrc((void*)p, 0, bytes, 0x20000); }
DEV void dma16(u32x4 srd, u32 voff, u32 soff, u32 lds) {
  asm volatile("s_mov_b32 m0, %3\n\ts_nop 0\n\tbuffer_load_dwordx4 %0, %1, %2 offen lds" :: "v"(voff), "s"(srd), "s"(soff), "s"(lds) : "memory");
}
template <int SO, int LO, int NT_ = 0>
DEV void dma16i(u32x4 srd, u32 voff, u32 soff, u32 lds) {
  u32 t;
  if (NT_) asm volatile("s_add_u32 m0, %4, %c6\n\ts_add_u32 %0, %3, %c5\n\tbuffer_load_dwordx4 %1, %2, %0 offen nt lds"
               : "=&s"(t) : "v"(voff), "s"(srd), "s"(soff), "s"(lds), "i"(SO), "i"(LO) : "memory", "scc");
  else
  asm volatile("s_add_u32 m0, %4, %c6\n\ts_add_u32 %0, %3, %c5\n\tbuffer_load_dwordx4 %1, %2, %0 offen lds"
               : "=&s"(t) : "v"(voff), "s"(srd), "s"(soff), "s"(lds), "i"(SO), "i"(LO) : "memory", "scc");
}
DEV void dma4(u32x4 srd, u32 voff, u32 lds) {
  asm volatile("s_mov_b32 m0, %2\n\ts_nop 0\n\tbuffer_load_dword %0, %1, 0 offen sc1 lds" :: "v"(voff), "s"(srd), "s"(lds) : "memory");
}
DEV void touch(u32 sink, u32x4 srd, u32 voff, u32 soff) {
  if (ABL & 8) return;
  if (NTT) { asm volatile("s_mov_b32 m0, %3\n\ts_nop 0\n\tbuffer_load_dword %0, %1, %2 offen nt lds" :: "v"(voff), "s"(srd), "s"(soff), "s"(sink) : "memory"); return; }
  asm volatile("s_mov_b32 m0, %3\n\ts_nop 0\n\tbuffer_load_dword %0, %1, %2 offen lds" :: "v"(voff), "s"(srd), "s"(soff), "s"(sink) : "memory");
}
DEV u64 ld_flag(const char* p) { return __hip_atomic_load((const GAS u64*)p, __ATOMIC_RELAXED, __HIP_MEMORY_SCOPE_AGENT); }
DEV void st_flag(char* p, u64 v) { __hip_atomic_store((GAS u64*)p, v, __ATOMIC_RELAXED, __HIP_MEMORY_SCOPE_AGENT); }
template <int SLEEP>
DEV void poll(const char* p, u64 tag) {
#pragma unroll 1
  for (int n = 0; n < SPIN_LIMIT; ++n) {
    if (__all(ld_flag(p) == tag)) break;
    if (SLEEP) __builtin_amdgcn_s_sleep(SLEEP);
  }
  asm volatile("" ::: "memory");
}
struct Args { const u16 *q, *k, *v, *sk, *h, *Dv, *x, *wq, *wo; u16* out; char* ws; };
typedef const __attribute__((address_space(4))) Args* argp;
#define FIN() __builtin_amdgcn_endpgm()
constexpr int U1 = NB + 7 * NE, U2 = U1 + 9 * NP, U3 = U2 + 9 * NG, U4 = U3 + 2 * NH;
static_assert(U4 <= 225 + 81, "warm-up units");
DEV void touch_unit(u32 sink, int n, int xcd, u32x4 sx, u32x4 sw, u32 vx, u32 vw) {
  int kc = n / 25, r = n - kc * 25;
  if (r < 9) {
    int row0 = 512 * (xcd >> 1) + (r < 8 ? 64 * r : -64);
    if (row0 >= 0) touch(sink, sx, r < 8 ? vx : (vx < 48 * 1152 ? 0x80000000u : vx), row0 * 1152 + kc * 128);
  } else {
    touch(sink, sw, vw, (kc * 192 + (r - 9) * 12) * 1152 + (xcd & 1) * 512);
  }
}

DEV void touch_run(argp A, u32 sink, int xcd, int lane, int n0, int n1, int dn, int gap = 1000) {
  const u32x4 so = mk_srd(A->wo, WO_BYTES), sx = mk_srd(A->x, ACT_BYTES), sw = mk_srd(A->wq, W_BYTES);
  const u32 vx = lane * 1152, vw = lane < 60 ? (lane / 5) * 1152 + (lane % 5) * 128 : 0x80000000u;
#pragma unroll 1
  for (int m = n0; m < n1; m += dn) {
    const int n = m < gap ? m : m + 9 * NP;
    if (n < 225) touch_unit(sink, n, xcd, sx, sw, vx, vw); else touch(sink, so, lane * 128, (n - 225) * 8192);
    WAIT_VM(PFQ);
  }
}

template <int ODD, int PI>
DEV void gemm1(DG f32x4 (&acc)[3][4], f32x4 (&mac)[2], f32x4& hac, u32 wA, u32 wM, u32 x0) {
  u32x2 wl[4], wh[4];
  u32x4 xf[2][4], xh;
  u32 wA2 = wA + 3 * WSLOT, wM2 = wM + 3 * WSLOT, x1 = x0 ^ 64;
  asm volatile("" : "+v"(wA2), "+v"(wM2), "+v"(x1));
  auto wadr = [&](int st, int c) -> u32 {
    const int sl = st % 5;
    return (c == 3 ? (sl < 3 ? wM : wM2) : (sl < 3 ? wA : wA2) + (c == 0 ? 0 : c == 1 ? WK : WV)) + (sl % 3) * WSLOT;
  };
  auto xadr = [&](int st, int t) -> u32 { return ((st & 1) ? x1 : x0) + ((st >> 1) % 3) * SLOT + t * 2048; };
  STAMP(4);
  bar_raw();
  STAMP(1);
  wl[0] = lds_tr(wadr(0, 0)); wh[0] = lds_tr(wadr(0, 0) + 768);
#pragma unroll
  for (int t = 0; t < 4; ++t) xf[0][t] = lds_r128(xadr(0, t));
  SB();
#pragma unroll
  for (int c = 1; c < 3; ++c) { wl[c] = lds_tr(wadr(0, c)); wh[c] = lds_tr(wadr(0, c) + 768); SB(); }
  const f32x4 Z = {0, 0, 0, 0};
#pragma unroll
  for (int st = 0; st < 18; ++st) {
#pragma unroll
    for (int n = 0; n < 14; ++n) {
      const int c = n >> 2, t = c == 3 ? 2 * ODD + (n & 1) : (n & 3);
      if (CWAIT && n == 0 && st) asm volatile("" :: "v"(xf[st & 1][3]));
      if (CWAIT && n == 4) asm volatile("" :: "v"(CWAIT == 2 ? wh[3] : wh[2]));
      SB();
      const u32x4 wv = {wl[c].x, wl[c].y, wh[c].x, wh[c].y}, xv = xf[st & 1][t];
      if ((ABL & 2) && st) {} else if (c == 3) mac[n & 1] = mma(wv, xv, st ? mac[n & 1] : Z);
      else acc[c][t] = c == 2 ? mma(xv, wv, st ? acc[c][t] : Z) : mma(wv, xv, st ? acc[c][t] : Z);
      SB();
      if (n == 0) wl[3] = lds_tr(wadr(st, 3));
      if (n == 1) wh[3] = lds_tr(wadr(st, 3) + 768);
      if (n == 2) xh = lds_r128(xadr(st, 8 - 4 * PI));
      if (st < 17) {
        if (n == 3) {
          if (!(ABL & 4)) bar_raw();
        }
        if (n == 4) wl[0] = lds_tr(wadr(st + 1, 0));
        if (n == 5) wh[0] = lds_tr(wadr(st + 1, 0) + 768);
        if (n >= 6 && n < 10) xf[(st + 1) & 1][n - 6] = lds_r128(xadr(st + 1, n - 6));
        if (n == 10 || n == 12) wl[(n - 8) >> 1] = lds_tr(wadr(st + 1, (n - 8) >> 1));
        if (n == 11 || n == 13) wh[(n - 8) >> 1] = lds_tr(wadr(st + 1, (n - 8) >> 1) + 768);
      }
      SB();
      if (n == 7 + 4 * PI) { hac = PI ? mma(xh, wv, st ? hac : Z) : mma(wv, xh, st ? hac : Z); SB(); }
    }
  }
}

DEV void load_wo(u32x4 swo, u32 lb, int lane, int cg, int bk) {
  const int r = lane >> 3;
  const u32 vo = r * 1152 + (((lane & 7) ^ (((r >> 1) & 3) << 1)) << 4) + cg * 128;
  const u32 src = ((bk + ROT(cg)) & 7) * 72 * 1152;
  const u32 dst = lb + L_WO + bk * 9216;
  dma16i<0, 0>(swo, vo, src, dst); dma16i<9216, 1024>(swo, vo, src, dst); dma16i<18432, 2048>(swo, vo, src, dst);
  dma16i<27648, 3072>(swo, vo, src, dst); dma16i<36864, 4096>(swo, vo, src, dst); dma16i<46080, 5120>(swo, vo, src, dst);
  dma16i<55296, 6144>(swo, vo, src, dst); dma16i<64512, 7168>(swo, vo, src, dst); dma16i<73728, 8192>(swo, vo, src, dst);
}
struct Relay { u32 r[4], w, mr, mr2, mw, vx, cp; u32x4 f[9]; };
DEV void relay_adr(Relay& R, u32 lb, int lane, int bk, int i2) {
  const int g = lane >> 4, q = (lane & 15) >> 2, p = lane & 3, row = 20 * (g >> 1) + 4 * (g & 1) + q;
#pragma unroll
  for (int ct = 0; ct < 4; ++ct) R.r[ct] = lb + L_WO + bk * 9216 + row * 128 + ((ct ^ ((row >> 1) & 3)) << 5) + 8 * p;
  R.w = lb + L_WO + bk * 9216 + lane * 16;
  R.mr = lb + L_WO + bk * 9216 + (16 + q) * 128 + ((g ^ (q >> 1)) << 5) + 8 * p; R.mr2 = R.mr ^ 64;
  R.mw = lb + L_BM + g * 1024 + (16 * bk + (lane & 15)) * 16;
  R.vx = WS_OX + i2 * 16384 + bk * 4096 + lane * 16;
  R.cp = WS_CHECK + (((i2 & 3) * 4 + ((lane >> 2) & 3)) * 8 + 2 * (i2 >> 2) + ((lane >> 4) & 1)) * 128 + (lane & 3) * 8;
  asm volatile("" : "+v"(R.r[0]), "+v"(R.r[1]), "+v"(R.r[2]), "+v"(R.r[3]), "+v"(R.w), "+v"(R.mr), "+v"(R.mr2), "+v"(R.mw), "+v"(R.vx), "+v"(R.cp));
}
DEV void relay_rd(Relay& R, int m) {
  const u32 off = m * 4 * 9216;
#pragma unroll
  for (int n = 0; n < 8; ++n) { const u32 a = R.r[(n & 3) ^ (2 * (n >> 2))] + off + (n >> 2) * 4608; R.f[n] = lds_tr2(a, a + 1024); SB(); }
  R.f[8] = lds_tr2(R.mr + off, R.mr2 + off + 4608);
  SB();
}
DEV void relay_wr(const Relay& R, int m) {
#pragma unroll
  for (int n = 0; n < 8; ++n) lds_w128(R.w + m * 4 * 9216 + (n >> 2) * 4608 + (n & 3) * 1024 + ((n >> 1) & 1) * 512, R.f[n]);
  lds_w128(R.mw + m * 4096, R.f[8]);
  SB();
}
DEV void gemm2_aux(DG char* ws, u32 lb, int lane, int w, int i2, int cg, u32 vx, int nap) {
  const rsrc_t rws = mk_rsrc(ws, WS_END);
  const int ln = (w - 8) * 32 + (lane & 31);
  const u32 vm = WS_OM + (i2 * 8 + (ln >> 4)) * 16384 + (ROT(cg) & 7) * 2048 + (ln & 15) * 128;
  bar();
  __builtin_amdgcn_s_setprio(3);
  if (nap) {
#pragma unroll 1
    for (int it = 0; it < SPIN_LIMIT; ++it) {
      asm volatile("" ::: "memory");
      if (__builtin_amdgcn_raw_buffer_load_b32(rws, WS_OX + i2 * 16384 + 252, 0, 16) != SENT) break;
      __builtin_amdgcn_s_sleep(HN);
    }
    if (HD) __builtin_amdgcn_s_sleep(HD);
  } else if (PDELAY) __builtin_amdgcn_s_sleep(PDELAY);
  u32x4 v[4];
#pragma unroll 1
  for (int it = 0; it < SPIN_LIMIT; ++it) {
    asm volatile("" ::: "memory");
    u32 chk = PSHARE ? __builtin_amdgcn_raw_buffer_load_b32(rws, vm, 0, 16) : 0;
#pragma unroll
    for (int n = 0; n < 4; ++n) v[n] = __builtin_amdgcn_raw_buffer_load_b128(rws, vx + n * 1024, 0, 16);
#pragma unroll
    for (int n = 0; n < 4; ++n) chk = max(max(max(chk, v[n].x), max(v[n].y, v[n].z)), v[n].w);
    if (__all(chk != SENT)) break;
    if (PGAP) __builtin_amdgcn_s_sleep(PGAP);
  }
  bar_raw();
#pragma unroll
  for (int n = 0; n < 4; ++n) lds_w128(lb + L_OX + (w - 8) * 4096 + n * 1024 + lane * 16, v[n]);
  bar();
}

DEV void gemm2(DG char* ws, u16* out, u32 lb, int lane, int w, int i2, int cg) {
  SB();
  bar_raw();
  SB();
  STAMP(5);
  const int g = lane >> 4, u = lane & 15;
  const rsrc_t rws = mk_rsrc(ws + WS_OM + (i2 * 8 + w) * 16384, (ABL & 64) ? 0 : 16384);
  u32 bw = lb + L_WO + lane * 16, bw2 = bw + 8 * 4608;
  asm volatile("" : "+v"(bw), "+v"(bw2));
  auto rdB = [&](int ks, int ct) -> u32x4 { return lds_r128((ks < 8 ? bw : bw2) + (ks & 7) * 4608 + ct * 1024 + (ct >> 1) * 512); };
  constexpr int DA_ = GDA, DB = GDB;
  u32x4 BM[2][4], B[DB][4], A[DA_];
#pragma unroll
  for (int n = 0; n < 8; ++n) BM[n >> 2][n & 3] = lds_r128(lb + L_BM + n * 1024 + lane * 16);
#pragma unroll
  for (int ks = 0; ks < DB; ++ks)
#pragma unroll
    for (int ct = 0; ct < 4; ++ct) B[ks][ct] = rdB(ks, ct);
  SB();
  u32 ax0 = lb + L_OX + (w >> 2) * 8192 + ((g + ROT(cg)) & 7) * 1024 + (w & 3) * 64 + u * 4;
  u32 ax1 = lb + L_OX + (w >> 2) * 8192 + ((g + ROT(cg) + 4) & 7) * 1024 + (w & 3) * 64 + u * 4;
  u32 sw_ = lb + L_W + w * 2304 + u * 144 + g * 8, sr_ = lb + L_W + w * 2304 + (lane >> 3) * 144 + (lane & 7) * 16;
  u32 oa = (128 * i2 + 32 * (w & 3) + 2 * (lane >> 3) + (w >> 2)) * 2304 + (lane & 7) * 16 + 1152 + cg * 128, ob = oa + 16 * 2304;
  const rsrc_t ro = mk_rsrc(out, OUT_BYTES);
  asm volatile("" : "+v"(ax0), "+v"(ax1), "+v"(oa), "+v"(ob), "+v"(sw_), "+v"(sr_));
  u32 so[8];
#pragma unroll
  for (int n = 0; n < 8; ++n) { so[n] = ((n + ROT(cg)) & 7) * 2048; asm volatile("" : "+s"(so[n])); }
  auto ldA = [&](int n) -> u32x4 { return __builtin_amdgcn_raw_buffer_load_b128(rws, lane * 16 + (n & 1) * 1024, so[n >> 1], AAUX); };
  SB();
  bar_raw();
  STAMP(6);
#pragma unroll
  for (int n = 0; n < NF; ++n) A[n] = ldA(n);
  SB();
  bar_raw();
  f32x4 ac0[4], acc[4];
  {
    const LAS u32 *pa = (const LAS u32*)ax0, *pb = (const LAS u32*)ax1;
    const u32x4 am = {pa[0], pa[64], pa[128], pa[192]}, an = {pb[0], pb[64], pb[128], pb[192]};
    SB();
#pragma unroll
    for (int n = NF; n < DA_; ++n) A[n] = ldA(n);
    SB();
#pragma unroll
    for (int ct = 0; ct < 4; ++ct) ac0[ct] = mma(BM[0][ct], am, f32x4{0, 0, 0, 0});
    SB();
#pragma unroll
    for (int ct = 0; ct < 4; ++ct) ac0[ct] = mma(BM[1][ct], an, ac0[ct]);
    SB();
  }
  SB();
  u32 chk = 0;
#pragma unroll
  for (int ks = 0; ks < ((ABL & 2048) ? 4 : 16); ++ks)
#pragma unroll
    for (int ct = 0; ct < 4; ++ct) {
      acc[ct] = mma(B[ks % DB][ct], A[ks % DA_], ks ? acc[ct] : ac0[ct]);
      SB();
      if (ks + DB < 16 && !(ABL & 128)) B[ks % DB][ct] = rdB(ks + DB, ct);
      if (ct < 2) chk = max(max(chk, A[ks % DA_][2 * ct]), A[ks % DA_][2 * ct + 1]);
      if (ct == 3 && ks + DA_ < 16) A[ks % DA_] = ldA(ks + DA_);
      if (ct == 3 && ((BMASK >> ks) & 1)) bar_raw();
      SB();
    }
  if (__builtin_expect(!__all(chk != SENT), 0)) {
#pragma unroll
    for (int ct = 0; ct < 4; ++ct) acc[ct] = ac0[ct];
#pragma unroll 1
    for (int ks = 0; ks < 16; ++ks) {
      u32x4 a;
#pragma unroll 1
      for (int it = 0; it < SPIN_LIMIT; ++it) {
        asm volatile("" ::: "memory");
        a = __builtin_amdgcn_raw_buffer_load_b128(rws, lane * 16, ((ks + 2 * ROT(cg)) & 15) * 1024, 16);
        if (__all(max(max(a.x, a.y), max(a.z, a.w)) != SENT)) break;
      }
#pragma unroll
      for (int ct = 0; ct < 4; ++ct) acc[ct] = mma(lds_r128(bw + ks * 4608 + ct * 1024 + (ct >> 1) * 512), a, acc[ct]);
    }
  }
  STAMP(7);
#pragma unroll
  for (int ct = 0; ct < 4; ++ct) lds_w64(sw_ + ct * 32, pk4(acc[ct]));
  const u32x4 v0 = lds_r128(sr_), v1 = lds_r128(sr_ + 8 * 144);
  __builtin_amdgcn_raw_buffer_store_b128(v0, ro, oa, 0, AUXB);
  __builtin_amdgcn_raw_buffer_store_b128(v1, ro, ob, 0, AUXB);
}

template <bool LOADER>
DEV u64 b_aux(DG argp A, u32 lb, int xcd, int slot, int cg, int i2, Relay& R, int w, int lane) {
  const int i = 4 * (xcd >> 1) + (slot >> 2), j = 4 * (xcd & 1) + (slot & 3);
  const int g = lane >> 4, q = (lane & 15) >> 2, p = lane & 3, u = lane & 15;
  const int hd = w & 3;
  const u32 off0 = (144 * j) & 63, rowb = lb + L_W + (8 * g + q) * 192, s0 = (g & 1) << 5;
  const u32 wA = rowb + ((off0 + 72 * (hd >> 1) + 40 * (hd & 1) + 8 * p) ^ s0);
  const u32 xr = lb + L_X + u * 128 + ((g ^ (u >> 1)) << 4);
  u64 tag = 0;
  {
    __builtin_amdgcn_s_setprio(3);
    auto clear = [&]() {
      if (!LOADER && OTB) touch(lb + L_SINK, mk_srd(A->out, OUT_BYTES), lane ? 0x80000000u : 0u, i2 * 128 * 2304 + 1152 + cg * 128);
      if (lane < 2) dma4(mk_srd(A->ws + WS_EPOCH, 8), lane * 4, lb + L_TAG + hd * 8);
      const rsrc_t rws = mk_rsrc(A->ws, WS_END);
      const u32x4 sent = {SENT, SENT, SENT, SENT};
      const u32 o0 = WS_OM + ((i * 8 + 2 * hd) * 16 + 2 * j) * 1024 + lane * 16;
#pragma unroll
      for (int n = 0; n < 4; ++n) __builtin_amdgcn_raw_buffer_store_b128(sent, rws, o0 + (n >> 1) * 16384 + (n & 1) * 1024, 0, 16);
      __builtin_amdgcn_raw_buffer_store_b64(u32x2{SENT, SENT}, rws, WS_OX + ((i * 2 + (hd >> 1)) * 32 + 4 * j) * 256 + (hd & 1) * 512 + lane * 8, 0, 16);
    };
    constexpr int NX = LOADER ? XL : 9 - 3 * XL;
    const int t0 = LOADER ? XL * (w - 9) : 3 * XL, r8 = lane >> 3;
    u32 vx[2][3];
    const u32 vx0 = r8 * 2304 + (((lane & 7) ^ (r8 >> 1)) << 4);
#pragma unroll
    for (int n = 0; n < NX; ++n) {
      const int t = t0 + n, hl = t >> 3;
      const u32 ro = (16 + (t & 3) * 32 + ((t >> 2) & 1) - 16 * hl * (i != 0)) * 1152;
      vx[0][n] = vx0 + ro; vx[1][n] = (vx0 ^ 64) + ro + (16 - 15 * hl) * 1152;
    }
    const u32x4 sxt = mk_srd((const char*)A->x + (128 * i - 16) * 1152, ACT_BYTES - (128 * i - 16) * 1152);
    u32 xk = 0, xl = lb + L_X + t0 * 2048;
    auto ldX = [&](int hf) {
      if (NX == 0) return;
      if (hf == 0) { dma16i<0, 0>(sxt, vx[0][0], xk, xl); dma16i<0, 2048>(sxt, vx[0][1], xk, xl); if (NX > 2) dma16i<0, 4096>(sxt, vx[0][2], xk, xl); }
      else {
        dma16i<0, 1024>(sxt, vx[1][0], xk, xl); dma16i<0, 3072>(sxt, vx[1][1], xk, xl); if (NX > 2) dma16i<0, 5120>(sxt, vx[1][2], xk, xl);
        xk += 128; xl = xl >= lb + L_X + 2 * SLOT ? xl - 2 * SLOT : xl + SLOT;
      }
    };
    STAMP(5);
    if (LOADER) {
      const int l20 = lane % 20, c20 = lane / 20, l11 = lane % 11, c11 = lane / 11;
      touch(lb + L_SINK, mk_srd(A->wq, W_BYTES), c20 < SW ? (c20 * 192 + l20 / 5) * 1152 + (l20 % 5) * 128 : 0x80000000u, (slot * 12 + (w - 9) * 4) * 1152 + (xcd & 1) * 512);
      const int row = 512 * (xcd >> 1) - 16 + slot * 33 + (w - 9) * 11 + l11;
      if (SX1 > SX0) touch(lb + L_SINK, mk_srd(A->x, ACT_BYTES), c11 < SX1 - SX0 && row >= 0 ? row * 1152 + (c11 + SX0) * 128 : 0x80000000u, 0);
    }
    ldX(0); ldX(1);
    const u32x4 swo = mk_srd(A->wo, WO_BYTES);
    if (LOADER) {
      const u32x4 sw = mk_srd(A->wq, W_BYTES);
      const int lw = w - 9;
      u32 vw[3];
#pragma unroll
      for (int n = 0; n < 3; ++n) {
        const int c = 64 * n + lane, row = c / 12, ch = c - row * 12;
        vw[n] = row * 3456 + ((ch ^ ((row >> 3) << 1)) << 4);
      }
      u32 wk = ((144 * j) & ~63) + lw * 1152, wl = lb + L_W + (lw == 0 ? 0 : lw == 1 ? WK : WV);
      auto ldW = [&]() {
        dma16i<0, 0>(sw, vw[0], wk, wl); dma16i<0, 1024>(sw, vw[1], wk, wl); dma16i<0, 2048>(sw, vw[2], wk, wl);
        dma16i<55296, 3072>(sw, vw[0], wk, wl); dma16i<55296, 4096>(sw, vw[1], wk, wl); dma16i<55296, 5120>(sw, vw[2], wk, wl);
        wk += 32 * 3456; wl = wl >= lb + L_W + 4 * WSLOT ? wl - 4 * WSLOT : wl + WSLOT;
      };
      ldW(); ldW(); ldX(0);
      STAMP(6);
      relay_adr(R, lb, lane, hd, i2);
#pragma unroll 1
      for (int k = 0; k < 7; ++k) {
        WAIT_VM(6 + NX); bar_raw(); ldX(1); ldW();
        WAIT_VM(6 + 2 * NX); bar_raw(); ldW(); ldX(0);
      }
      WAIT_VM(6 + NX); bar_raw(); ldX(1); ldW();
      WAIT_VM(6 + 2 * NX); bar_raw(); ldW();
      WAIT_VM(6); bar_raw(); clear();
      WAIT_VM(6); bar_raw();
    } else {
      if (NX) __builtin_amdgcn_s_sleep(XS);
      ldX(0);
      const u32 wM = rowb + ((off0 + 72 * (p >> 1) + 32) ^ s0) + ((p & 1) ? WV : WK);
      f32x4 hm = {0, 0, 0, 0};
      u32x4 a = {0, 0, 0, 0}, bm = a;
      u32 wr = 0, xs = 0;
      auto halo = [&](int odd) {
        hm = mma(bm, a, hm);
        SB();
        a = lds_r128(((xr + 16384) ^ (odd * 64)) + xs); bm = lds_tr2(wM + wr, wM + wr + 768);
        wr = wr == 4 * WSLOT ? 0 : wr + WSLOT; if (odd) xs = xs == 2 * SLOT ? 0 : xs + SLOT;
        SB();
      };
      relay_adr(R, lb, lane, hd, i2);
      asm volatile("" : "+v"(hm), "+v"(a), "+v"(bm));
      STAMP(12);
      if (NX) WAIT_VM(NX);
#pragma unroll 1
      for (int k = 0; k < 8; ++k) {
        bar_raw();
        ldX(1); halo(0);
        bar_raw(); if (k < 7) { ldX(0); halo(1); WAIT_VM(NX); } else { halo(1); WAIT_VM(0); }
      }
      bar_raw(); STAMP(13); clear(); halo(0);
      bar_raw(); halo(1);
      hm = mma(bm, a, hm);
      LAS u32* e = (LAS u32*)(lb + E_HX + (g >> 1) * 256 + u * 16 + (g & 1) * 4);
      e[0] = pk(hm[0], hm[1]); e[2] = pk(hm[2], hm[3]);
    }
    STAMP(2);
    load_wo(swo, lb, lane, cg, hd);
    WAIT_VM(9);
    { const u32x2 e = lds_r64(lb + L_TAG + hd * 8); tag = (((u64)e.y << 32) | e.x) + 1; }
    if (lane == 0) st_flag(A->ws + WS_CHECK + blockIdx.x * 128 + hd * 8, tag);
    STAMP(3);
    bar();
    if (RFIRST) { WAIT_VM(1); relay_rd(R, 0); }
    load_wo(swo, lb, lane, cg, hd + 4);
    __builtin_amdgcn_s_setprio(RPRIO);
  }
  return tag;
}

DEV void b_main(DG argp A, u32 lb, int xcd, int slot, int w, int lane) {
  const int i = 4 * (xcd >> 1) + (slot >> 2), j = 4 * (xcd & 1) + (slot & 3);
  const int g = lane >> 4, q = (lane & 15) >> 2, p = lane & 3, u = lane & 15;
  const int hd = w & 3;
  const u32 off0 = (144 * j) & 63, rowb = lb + L_W + (8 * g + q) * 192, s0 = (g & 1) << 5;
  const u32 wA = rowb + ((off0 + 72 * (hd >> 1) + 40 * (hd & 1) + 8 * p) ^ s0);
  const u32 xr = lb + L_X + u * 128 + ((g ^ (u >> 1)) << 4);
  char* ws = A->ws;
  const int pi = w >> 2;
  const rsrc_t rws = mk_rsrc(ws, WS_END);
  u32 oadr = WS_OM + ((i * 8 + pi * 4 + (lane >> 5)) * 16 + 2 * j + (hd >> 1)) * 1024 + (hd & 1) * 512 + (lane & 31) * 16;
  u32 xadr = WS_OX + ((i * 2 + pi) * 32 + j * 4 + hd) * 256 + lane * 4;
  lds_w128(lb + E_MX + (w * 64 + lane) * 16, u32x4{0, 0, 0, 0});
  if (w == 0 && lane < 16) lds_w128(lb + E_MX + 8192 + lane * 16, u32x4{0, 0, 0, 0});
  u32 mC[2] = {0, 0}, mP[2] = {0, 0};
#pragma unroll
  for (int r = 0; r < 4; ++r) {
    const int t = 4 * g + r;
    mC[r >> 1] |= (t <= u && t + 7 >= u) ? 0xffffu << (16 * (r & 1)) : 0u;
    mP[r >> 1] |= (t >= u + 9) ? 0xffffu << (16 * (r & 1)) : 0u;
  }
  const u32 eb = lb + E_MX + (pi * 2 + (hd >> 1)) * 4 * ESTR, hb = lb + E_HX + (hd >> 1) * 256 + 8 * (hd & 1);
  u32 aW = eb + (hd & 1) * 2 * ESTR + u * 16 + (g == 0 ? 0 : g == 1 ? 256 : 260);
  u32 aQ = g == 0 ? eb + u * 16 + 8 * (hd & 1) : eb + 512;
  u32 aK = eb + 256 + u * 16 + 8 * (hd & 1);
  u32 aF = eb + p * ESTR + 256 + (4 * g + q) * 16 + 8 * (hd & 1);
  u32 aE = p ? aF - ESTR : hb + (8 * pi + 4 * (g & 1) + q) * 16;
  u32 aH = lb + L_EX + hd * 1024 + pi * 512 + lane * 8;
  asm volatile("" : "+v"(aH));
  u32 aHK = lb + L_EX + hd * 1024 + (g * 16 + (u & 7) + 8 * pi) * 8;
  u32 aHX = hb + (8 * pi + (u & 7)) * 16;
  u32 aHV = lb + L_EX + hd * 1024 + 512 + (((g & 1) + 2 * pi) * 16 + u) * 8;
  asm volatile("" : "+v"(mC[0]), "+v"(mC[1]), "+v"(mP[0]), "+v"(mP[1]), "+v"(aW), "+v"(aQ), "+v"(aK), "+v"(aF), "+v"(aE), "+v"(aHK), "+v"(aHX), "+v"(aHV), "+v"(oadr), "+v"(xadr));
  const float nb1 = i == 0 ? -__builtin_inff() : 0.f;
  f32x4 nb = {nb1, nb1, nb1, nb1};
  u32 oadr2 = oadr + 32768;
  asm volatile("s_waitcnt lgkmcnt(0)" : "+v"(nb), "+v"(oadr2) :: "memory");
  u64 lp[4] = {lm_prev(0), lm_prev(1), lm_prev(2), lm_prev(3)}, ll[4] = {lm_low(0), lm_low(1), lm_low(2), lm_low(3)};
  u64 le = 0x0000ffff0000ffffull, lh = 0xffffffffull;
  asm volatile("" : "+s"(lp[0]), "+s"(lp[1]), "+s"(lp[2]), "+s"(lp[3]), "+s"(ll[0]), "+s"(ll[1]), "+s"(ll[2]), "+s"(ll[3]), "+s"(le), "+s"(lh) :: "vcc");
  f32x4 acc[3][4], mac[2], hac;
  {
    const u32 wM = rowb + ((off0 + 72 * (hd >> 1) + 32) ^ s0) + (p == 0 ? 0 : p == 1 ? WK : WV);
    if (pi) { if (hd & 1) gemm1<1, 1>(DA acc, mac, hac, wA, wM, xr + 8192); else gemm1<0, 1>(DA acc, mac, hac, wA, wM, xr + 8192); }
    else { if (hd & 1) gemm1<1, 0>(DA acc, mac, hac, wA, wM, xr); else gemm1<0, 0>(DA acc, mac, hac, wA, wM, xr); }
  }
  STAMP(2);
  lds_w64(aH, pk4(hac));
#pragma unroll
  for (int n = 0; n < 2; ++n) { LAS u32* e = (LAS u32*)(aW + n * ESTR); e[0] = pk(mac[n][0], mac[n][1]); e[2] = pk(mac[n][2], mac[n][3]); }
  SB();
  u32x2 P[2][4];
  u32x4 V[4];
#pragma unroll
  for (int t = 0; t < 4; ++t) {
    P[0][t] = pk4(acc[0][t]); P[1][t] = pk4(acc[1][t]);
    u32x2 v = pk4(acc[2][t]), v2 = v;
    asm volatile("" : "+v"(P[0][t]), "+v"(P[1][t]), "+v"(v));
    if (t < 3) { asm volatile("" : "+v"(v2)); V[t + 1].x = v2.x; V[t + 1].y = v2.y; }
    V[t].z = v.x; V[t].w = v.y;
  }
  SB();
  bar();
  STAMP(9);
  const f32x4 Z = {0, 0, 0, 0};
  u32x4 Q[4], K[5];
#pragma unroll
  for (int t = 3; t >= 0; --t) {
    const u32x2 kx = lds_r64v(aK + t * ESTR), qx = lds_r64v(aQ + t * ESTR);
    Q[t] = u32x4{P[0][t].x, P[0][t].y, qx.x, qx.y}; K[t + 1] = u32x4{P[1][t].x, P[1][t].y, kx.x, kx.y};
  }
  { const u32x2 a = lds_r64v(aHK), b = lds_r64v(aHX), c = lds_r64v(aHV); K[0] = u32x4{a.x, a.y, b.x, b.y}; V[0].x = c.x; V[0].y = c.y; }
  const u32x4 VX = lds_tr2(aE, aF);
  SB();
  f32x4 sc[4], sp[4];
#pragma unroll
  for (int t = 3; t >= 0; --t) { sc[t] = mma(K[t + 1], Q[t], Z); sp[t] = mma(K[t], Q[t], t ? Z : nb); SB(); }
  float M[4][4], X[4], Y[4];
#pragma unroll
  for (int t = 3; t >= 2; --t)
#pragma unroll
    for (int r = 0; r < 4; ++r) M[t][r] = lself(lp[r], sp[t][r], sc[t][r]);
  SB();
#pragma unroll
  for (int r = 0; r < 4; ++r) swap32f(M[2][r], M[3][r]);
  SB();
#pragma unroll
  for (int t = 1; t >= 0; --t)
#pragma unroll
    for (int r = 0; r < 4; ++r) M[t][r] = lself(lp[r], sp[t][r], sc[t][r]);
  SB();
#pragma unroll
  for (int r = 0; r < 4; ++r) swap32f(M[0][r], M[1][r]);
  SB();
#pragma unroll
  for (int r = 0; r < 4; ++r) Y[r] = lself(ll[r], M[2][r], M[3][r]);
#pragma unroll
  for (int r = 0; r < 4; ++r) X[r] = lself(ll[r], M[0][r], M[1][r]);
  SB();
#pragma unroll
  for (int r = 0; r < 4; ++r) swap16f(X[r], Y[r]);
  SB();
  const float cs = 0.23570226039551584f * 1.4426950408889634f;
  const float mx = fmx3(fmx3(X[0], Y[0], X[1]), fmx3(Y[1], X[2], Y[2]), fmx3(X[3], Y[3], Y[3]));
  const float nm = -mx * cs;
#pragma unroll
  for (int r = 0; r < 4; ++r) { X[r] = __builtin_amdgcn_exp2f(__builtin_fmaf(X[r], cs, nm)); Y[r] = __builtin_amdgcn_exp2f(__builtin_fmaf(Y[r], cs, nm)); }
  f32x2 e0 = {X[0], X[1]}, e1 = {X[2], X[3]}, e2 = {Y[0], Y[1]}, e3 = {Y[2], Y[3]};
  const f32x2 es = (e0 + e2) + (e1 + e3);
  const float rs = __builtin_amdgcn_rcpf(es.x + es.y);
  SB();
  e0 *= rs; e2 *= rs; e1 *= rs; e3 *= rs;
  SB();
  u32 G[4][2];
  G[0][0] = pk(e0.x, e0.y); G[2][0] = pk(e2.x, e2.y); G[0][1] = pk(e1.x, e1.y); G[2][1] = pk(e3.x, e3.y);
  SB();
  swap16(G[0][0], G[2][0]); swap16(G[0][1], G[2][1]);
  SB();
  G[1][0] = G[0][0]; G[1][1] = G[0][1]; G[3][0] = G[2][0]; G[3][1] = G[2][1];
  asm volatile("" : "+v"(G[1][0]), "+v"(G[1][1]), "+v"(G[3][0]), "+v"(G[3][1]));
  SB();
  swap32(G[0][0], G[1][0]); swap32(G[0][1], G[1][1]); swap32(G[2][0], G[3][0]); swap32(G[2][1], G[3][1]);
  SB();
  f32x4 o[4], ox[4];
  u32x4 Pm[4];
#pragma unroll
  for (int t = 0; t < 5; ++t) {
    if (t < 4) Pm[t] = u32x4{G[t][0] & mP[0], G[t][1] & mP[1], G[t][0] & mC[0], G[t][1] & mC[1]};
    SB();
    if (t) o[t - 1] = mma(V[t - 1], Pm[t - 1], Z);
    if (t == 3) ox[0] = mma(VX, Pm[0], Z);
    SB();
  }
  u32 O[4][2];
#pragma unroll
  for (int t = 0; t < 4; ++t) {
    if (t < 3) ox[t + 1] = mma(VX, Pm[t + 1], Z);
    SB();
    O[t][0] = pk(o[t][0], o[t][1]); O[t][1] = pk(o[t][2], o[t][3]);
    SB();
  }
  swap32(O[0][0], O[1][0]); swap32(O[0][1], O[1][1]);
  __builtin_amdgcn_raw_buffer_store_b128(u32x4{live(O[0][0]), live(O[0][1]), live(O[1][0]), live(O[1][1])}, rws, oadr, 0, 16);
  swap32(O[2][0], O[3][0]); swap32(O[2][1], O[3][1]);
  __builtin_amdgcn_raw_buffer_store_b128(u32x4{live(O[2][0]), live(O[2][1]), live(O[3][0]), live(O[3][1])}, rws, oadr2, 0, 16);
  SB();
  {
    const float l0 = lself(le, ox[0][2], ox[1][2]), l1 = lself(le, ox[0][3], ox[1][3]), h0 = lself(le, ox[2][2], ox[3][2]), h1 = lself(le, ox[2][3], ox[3][3]);
    __builtin_amdgcn_raw_buffer_store_b32(live(pk(lself(lh, l0, h0), lself(lh, l1, h1))), rws, xadr, 0, 16);
  }
  STAMP(3);
}

DEV u32 kv_adr(int a, int oc, int lane) { return (lane >> 3) * 32768 + (((lane & 7) ^ ((oc >> 1) & 7)) << 4) + WS_KVX + a * 262144 + oc * 128; }
DEV void p0_issue(argp A, u32 lb, int a, int blk, int w, int lane) {
  const u32x4 sk_ = mk_srd(A->k, ACT_BYTES), sv = mk_srd(A->v, ACT_BYTES);
  const int s0 = 256 * blk + 32 * w, r8 = lane >> 3;
  const u32 st = lb + C_P0 + w * 9216;
  const u32 vo = r8 * 1152 + (((lane & 7) ^ (((r8 >> 1) & 1) << 2)) << 4);
  const u32 so = s0 * 1152 + a * 128;
  dma16(sk_, lane < 32 ? 0x80000000u : vo, s0 ? so - 8 * 1152 : so, st);
  dma16i<0, 1024, NTKV>(sk_, vo, so, st); dma16i<0, 5120, NTKV>(sv, vo, so, st); dma16i<9216, 2048, NTKV>(sk_, vo, so, st); dma16i<9216, 6144, NTKV>(sv, vo, so, st);
  dma16i<18432, 3072, NTKV>(sk_, vo, so, st); dma16i<18432, 7168, NTKV>(sv, vo, so, st); dma16i<27648, 4096, NTKV>(sk_, vo, so, st); dma16i<27648, 8192, NTKV>(sv, vo, so, st);
}
DEV void p0_finish(char* ws, u32x2 wv, u32 lb, int a, int blk, int w, int lane) {
  const int s0 = 256 * blk + 32 * w;
  const u32 st = lb + C_P0 + w * 9216;
  const u32 w10 = __builtin_amdgcn_alignbit(wv.x, wv.x, 16), w32 = __builtin_amdgcn_alignbit(wv.y, wv.y, 16);
  const int g = lane >> 4, q = (lane & 15) >> 2, p = lane & 3;
  const u32 ra = st + q * 128 + ((32 * g + 8 * p) ^ ((q >> 1) << 6));
  u32 Kd[18], Vd[16];
#pragma unroll
  for (int n = 0; n < 9; ++n) { u32x2 t = lds_tr(ra + 512 + n * 512); Kd[2 * n] = t.x; Kd[2 * n + 1] = t.y; }
#pragma unroll
  for (int n = 0; n < 8; ++n) { u32x2 t = lds_tr(ra + 5120 + n * 512); Vd[2 * n] = t.x; Vd[2 * n + 1] = t.y; }
  if (s0 == 0) { Kd[0] = 0; Kd[1] = 0; }
  const rsrc_t rws = mk_rsrc(ws, WS_END);
  u32 ko = __builtin_amdgcn_alignbit(Kd[1], Kd[0], 16);
#pragma unroll
  for (int n = 0; n < 4; ++n) {
    u32x4 o;
#pragma unroll
    for (int e = 0; e < 4; ++e) {
      const int m = 4 * n + e;
      const u32 kn = __builtin_amdgcn_alignbit(Kd[m + 2], Kd[m + 1], 16);
      const float ev = dot2(kn, w10, dot2(ko, w32, 0.f)) * blo(Vd[m]);
      const float od = dot2(Kd[m + 2], w10, dot2(Kd[m + 1], w32, 0.f)) * bhi(Vd[m]);
      ko = kn;
      o[3 - e] = pk(od, ev);
    }
    const int oc = 32 * blk + 4 * w + n;
    __builtin_amdgcn_raw_buffer_store_b128(o, rws, (ABL & 16) ? 0x80000000u + o.x % 7 : kv_adr(a, oc, lane), 0, 16);
  }
}

DEV void h_issue(argp A, u32 lb, int late, int ch, int w, int lane) {
  const u32x4 sh = mk_srd(A->h, ACT_BYTES);
  const u32 hb = lb + C_H + w * HSTR, s1 = ch * 4096 + (late ? 32768 : 2048), h1 = hb + (late ? 4192 : 2048);
  dma16i<0, 32, NTH>(sh, lane * 16, ch * 4096, hb); dma16i<1024, 1056, NTH>(sh, lane * 16, ch * 4096, hb);
  dma16i<0, 32, NTH>(sh, lane * 16, s1, h1); dma16i<1024, 1056, NTH>(sh, lane * 16, s1, h1);
}
template <bool EXTRA>
DEV void conv_main(DG argp A, const u32 (&dv)[2], u32 lb, int a, int sub, int w, int lane, bool p0) {
  const int g = lane >> 4, r = lane & 15;
  const int ch0 = a * 64 + sub * 8;
  char* ws = A->ws;
  const int blk = EXTRA ? (w & 3) : w, set = EXTRA ? (w >> 2) : 0;
  constexpr u32 OOFF = EXTRA ? 2112 : 4160;
  const u32 hb = lb + C_H + w * HSTR;
  const u32 aa = hb + ((r & 1) ? 0 : OOFF) + ((r >> 1) + 1 + 4 * g) * 4;
  auto baddr = [&](int b) -> u32 { const int o = 33 + 2 * r - g - 4 * b; return lb + C_X + o * 128 + ((w ^ ((o >> 1) & 7)) << 4); };
  auto boff = [](int c) -> u32 { return EXTRA ? (c >> 2) * 20480 + (c & 3) * 4096 : c * 4096; };
  auto aoff = [](int c) -> u32 { return EXTRA ? (c >> 2) * 4192 + (c & 3) * 512 : c * 512; };
  constexpr int NA = EXTRA ? 8 : 4;
  f32x4 acc[NA];
  u32x2 Al[8], Ah[8];
  u32x4 B[8];
  u32 ab[4], bb4[4];
#pragma unroll
  for (int n = 0; n < 4; ++n) { ab[n] = aa + aoff(2 * n); bb4[n] = baddr(4 + n); asm volatile("" : "+v"(ab[n]), "+v"(bb4[n])); }
  auto ld = [&](int c, int part, int b) {
    if (b > 7) return;
    const LAS u32* pa = (const LAS u32*)(ab[c >> 1] + (c & 1) * 512 + b * 64);
    const int cb = EXTRA ? c ^ 3 : 7 - c;
    if (part == 0) Al[c] = u32x2{pa[0], pa[1]};
    else if (part == 1) Ah[c] = u32x2{pa[2], pa[3]};
    else B[cb] = lds_r128(bb4[b & 3] + (b < 4 ? 2048 : 0) + boff(cb));
  };
  const int rr = w * 128 + (EXTRA ? lane & ~1 : lane), cc = EXTRA ? (lane & 1) * 16 : 0;
  u32 zw = hb + r * 32 + g * 8, za = lb + C_H + (r >> 2) * HSTR + (w * 128 + 16 * g) * 2 + 8 * (lane & 3), qa = lb + C_Q + (EXTRA ? rr * 32 + cc : rr * 16);
  u32 oa[2] = {((EXTRA ? 0 : 1024) + rr) * 2304u + ch0 * 2 + cc, ((EXTRA ? 64 : 1088) + rr) * 2304u + ch0 * 2 + cc};
  u64 odd = 0xaaaaaaaaaaaaaaaaull, outp = (u64)A->out;
  asm volatile("" : "+v"(zw), "+v"(za), "+v"(qa), "+v"(oa[0]), "+v"(oa[1]));
  asm volatile("" : "+s"(odd), "+s"(outp) :: "vcc");
  const rsrc_t ro = mk_rsrc((void*)outp, OUT_BYTES);
  u32x4 AD[2];
  {
    const int jd = 15 - r - 8 * g;
#pragma unroll
    for (int n = 0; n < 2; ++n) {
      const u32 d = dv[n] << (16 * (jd & 1));
      AD[n] = u32x4{(jd >> 1) == 0 ? d : 0u, (jd >> 1) == 1 ? d : 0u, (jd >> 1) == 2 ? d : 0u, (jd >> 1) == 3 ? d : 0u};
    }
    asm volatile("" : "+v"(AD[0]));
    if (EXTRA) asm volatile("" : "+v"(AD[1]));
  }
  SB();
  if (p0) WAIT_VM(4);
  else { __builtin_amdgcn_s_sleep(HS1); h_issue(A, lb, EXTRA, ch0 + w, w, lane); WAIT_VM(0); }
  {
    constexpr u32 HS = EXTRA ? 4192 : 2048;
    u32x4 H[4];
    u32 pv[4];
#pragma unroll
    for (int n = 0; n < 4; ++n) { const u32 o = hb + (n >> 1) * HS + (n & 1) * 1024 + 32 + lane * 16; H[n] = lds_r128(o); pv[n] = lds_r32(o - 4); }
#pragma unroll
    for (int n = 0; n < 4; ++n)
      lds_w128(hb + OOFF + (n >> 1) * HS + (n & 1) * 1024 + 32 + lane * 16,
               u32x4{__builtin_amdgcn_alignbit(H[n].x, pv[n], 16), __builtin_amdgcn_alignbit(H[n].y, H[n].x, 16), __builtin_amdgcn_alignbit(H[n].z, H[n].y, 16), __builtin_amdgcn_alignbit(H[n].w, H[n].z, 16)});
  }
  SB();
  WAIT_VM(0);
  STAMP(12);
  bar_raw();
  const char* fp = ws + WS_CHECK + ((16 + a) * 8 + blk) * 128;
  const u64 f0 = EARLYF ? ld_flag(fp) : 0;
  const u32x2 e = lds_r64(lb + L_TAG);
  SB();
  lds_w64(lb + C_X + (w * 64 + lane) * 8, u32x2{0, 0});
  if (EXTRA) lds_w64(lb + C_X + 20480 + (w * 64 + lane) * 8, u32x2{0, 0});
#pragma unroll
  for (int c = 0; c < 6; ++c) { ld(c, 0, 0); ld(c, 1, 0); }
  SB();
  const rsrc_t rws = mk_rsrc(ws + WS_KVX + (a * 8 + sub + set) * 32768 + blk * 4096, 4096);
  { const u64 tag = ((u64)e.y << 32) | e.x; if (!EARLYF || !__all(f0 == tag)) poll<KGAP>(fp, tag); }
  asm volatile("" ::: "memory");
  STAMP(2);
  u32x4 xv[4];
#pragma unroll
  for (int n = 0; n < 4; ++n) xv[n] = __builtin_amdgcn_raw_buffer_load_b128(rws, lane * 16 + n * 1024, 0, 16);
  if (OWNPOLL) { SB(); bar_raw(); SB(); }
#pragma unroll
  for (int n = 0; n < 4; ++n) lds_w128(lb + C_X + set * 20480 + 4096 + blk * 4096 + n * 1024 + lane * 16, xv[n]);
  bar();
  STAMP(4);
#pragma unroll
  for (int c = 0; c < 8; ++c) { ld(c, 2, 0); SB(); }
#pragma unroll
  for (int c = 0; c < NA; ++c) { const int t = EXTRA ? c ^ 3 : 3 - c; acc[t] = mma(AD[EXTRA ? t >> 2 : 0], B[EXTRA ? t : 4 + t], f32x4{0, 0, 0, 0}); SB(); }
#pragma unroll
  for (int b = 0; b < ((ABL & 1024) ? 6 : 8); ++b) {
    int slot_ = 0;
#pragma unroll
    for (int c = 0; c < 8; ++c) {
      const int thi = EXTRA ? (c | 3) : 7, tlo = EXTRA ? c : (c > 4 ? c : 4);
#pragma unroll
      for (int t = thi; t >= tlo; --t) {
        if (VW && !EXTRA && b) {
          if (slot_ == 0) asm volatile("" :: "v"(B[3]));
          if (slot_ == 8) asm volatile("" :: "v"(B[1]));
          if (slot_ == 16) asm volatile("" :: "v"(Ah[5]));
          if (slot_ == 20) asm volatile("" :: "v"(Ah[7]));
          SB();
        }
        const u32x4 Av = {Al[c].x, Al[c].y, Ah[c].x, Ah[c].y};
        if (EXTRA) acc[t] = mma(Av, B[(c & 4) + t - c], acc[t]); else acc[t - 4] = mma(Av, B[t - c], acc[t - 4]);
        SB();
        const int n = slot_++;
        if (t == thi) ld(c, 2, b + 1);
        if (!EXTRA) {
          if (n == 1 || n == 2) ld(6, n - 1, b);
          else if (n == 3) { if (b) ld(5, 1, b); }
          else if (n == 7 || n == 11) ld(7, n >> 3, b);
          else if (n == 24) ld(5, 0, b + 1);
          else if (n < 23 && (n & 3) && (n & 3) < 3) ld((n >> 2) - 1, (n & 3) - 1, b + 1);
        } else {
          if (n == 1) { ld(6, 0, b); ld(6, 1, b); }
          else if (n == 2 || n == 3) ld(7, n - 2, b);
          else if (n == 5 || n == 6) ld(0, n - 5, b + 1);
          else if (n == 8) { ld(1, 0, b + 1); ld(1, 1, b + 1); }
          else if (n == 11) { ld(2, 0, b + 1); ld(2, 1, b + 1); }
          else if (n == 12 || n == 13) ld(3, n - 12, b + 1);
          else if (n == 15 || n == 16) ld(4, n - 15, b + 1);
          else if (n == 18) { ld(5, 0, b + 1); ld(5, 1, b + 1); }
        }
        SB();
      }
    }
  }
  STAMP(5);
#pragma unroll
  for (int t = 0; t < NA; ++t) lds_w64(zw + t * 512, pk4(acc[t]));
  bar();
  STAMP(6);
  constexpr int NS = EXTRA ? 2 : 1;
  u32x4 z[2][NS], qq[2][NS];
#pragma unroll
  for (int n = 0; n < 2; ++n)
#pragma unroll
    for (int s = 0; s < NS; ++s) { z[n][s] = lds_tr2(za + n * 128 + s * 2048, za + n * 128 + s * 2048 + 4 * HSTR); qq[n][s] = lds_r128(qa + n * 1024 * NS + s * 32); SB(); }
#pragma unroll
  for (int n = 0; n < 2; ++n) {
    if (EXTRA) {
#pragma unroll
      for (int e = 0; e < 4; ++e) {
        const u32 t0 = z[n][0][e], t1 = z[n][NS - 1][e];
        z[n][0][e] = lsel(odd, __builtin_amdgcn_update_dpp(t0, t1, 0xa0, 0xf, 0xf, false), t0);
        z[n][NS - 1][e] = lsel(odd, t1, __builtin_amdgcn_update_dpp(t1, t0, 0xf5, 0xf, 0xf, false));
      }
    }
#pragma unroll
    for (int s = 0; s < NS; ++s) {
      u32x4 o;
#pragma unroll
      for (int e = 0; e < 4; ++e) o[e] = pk(blo(z[n][s][e]) * blo(qq[n][s][e]), bhi(z[n][s][e]) * bhi(qq[n][s][e]));
      __builtin_amdgcn_raw_buffer_store_b128(o, ro, oa[n] + s * 2304, 0, AUXA);
      SB();
    }
  }
}

DEV int late_role(int xcd, int slot, int& ca, int& sub) {
  ca = xcd;
  if (slot < 24) { sub = slot - 16; return 0; }
  if (slot >= 30) { sub = 0; return 3; }
  if (xcd < 2) {
    if (slot < 28) { ca = 8; sub = 4 * xcd + slot - 24; return 0; }
    sub = 2 * (slot - 28); return 1;
  }
  if (slot >= 26) { sub = 2 * (slot - 26); return 1; }
  ca = xcd < 4 ? xcd - 2 : 8;
  sub = (xcd == 4 ? 0 : 4) + 2 * (slot - 24);
  return xcd < 6 ? 1 : 2;
}

DEV void nb_main(DG argp A, u32 lb, int xcd, int slot, int w, int lane) {
  const bool p0 = slot <= 24;
  char* ws = A->ws;
  const u32 sm = lb + C_S + w * 1536;
  if (p0) {
    p0_issue(A, lb, slot - 16, xcd, w, lane);
    dma16(mk_srd(A->sk, 4608), lane < 32 ? lane * 16 : 0x80000000u, (slot - 16) * 512, sm);
  }
  int ca, sub;
  const int late = late_role(xcd, slot, ca, sub);
  const int ch = late == 2 ? 0 : ca * 64 + sub * 8 + w;
  dma4(mk_srd(A->Dv, 1152), lane < 2 ? (ch >> 1) * 4 + lane * 16 : 0x80000000u, sm + 1280);
  if (lane < (late ? 8 : 4)) lds_w128(lb + C_H + w * HSTR + (lane >> 2) * 4192 + ((lane >> 1) & 1) * (late ? 2112 : 4160) + (lane & 1) * 16, u32x4{0, 0, 0, 0});
  if (p0) {
    bar_raw();
    h_issue(A, lb, late, ch, w, lane);
    WAIT_VM(4);
    STAMP(1);
    p0_finish(ws, lds_r64(sm + lane * 8), lb, slot - 16, xcd, w, lane);
    STAMP(9);
  } else WAIT_VM(0);
  const u32x2 dd = lds_r64(sm + 1280);
  const u32 dv[2] = {(dd.x >> (16 * (w & 1))) & 0xffffu, (dd.y >> (16 * (w & 1))) & 0xffffu};
  if (late == 0) conv_main<false>(DA A, dv, lb, ca, sub, w, lane, p0);
  else if (late == 1) conv_main<true>(DA A, dv, lb, ca, sub, w, lane, p0);
  else { WAIT_VM(0); bar_raw(); }
  FIN();
}

DEV u64 nb_aux(DG argp A, u32 lb, int xcd, int slot, int i2, int w, int lane) {
  const u32 sink = lb + L_SINK;
  u64 tag = 0;
  if (slot <= 24) bar_raw();
  if (w >= 9 && w < 9 + NPW) {
    if (slot <= 24) __builtin_amdgcn_s_sleep(HS0);
    touch_run(A, sink, xcd, lane, slot <= 24 ? PB + slot - 16 + 9 * (w - 9) : NB + slot - 25 + 7 * (w - 9), slot <= 24 ? PB + 9 * NP : U1, slot <= 24 ? 9 * NPW : 7 * NPW, slot <= 24 ? 1000 : PB);
    if (slot >= 30) touch_run(A, sink, xcd, lane, U3 + slot - 30 + 2 * (w - 9), U4, 2 * NPW);
    STAMP(1);
  } else if (w == 8) {
    tag = ld_flag(A->ws + WS_EPOCH) + 1;
    lds_w64(lb + L_TAG, u32x2{(u32)tag, (u32)(tag >> 32)});
    asm volatile("s_waitcnt lgkmcnt(0)" ::: "memory");
    if (slot > 24 && lane < 4) st_flag(A->ws + WS_CHECK + blockIdx.x * 128 + lane * 8, tag);
  }
  int ca, sub;
  const int late = late_role(xcd, slot, ca, sub);
  if (w >= 9 && slot > 24) WAIT_VM(PFW);
  bar_raw();
  if (w == 8) {
    if (slot <= 24 && lane < 4) st_flag(A->ws + WS_CHECK + blockIdx.x * 128 + lane * 8, tag);
    if (late < 2 && !OWNPOLL) poll<KGAP>(A->ws + WS_CHECK + ((16 + ca) * 8 + (lane & (late ? 3 : 7))) * 128, tag);
  }
  if (NG && w == 10 && slot <= 24) touch_run(A, sink, xcd, lane, U2 + slot - 16, U3, 9);
  if (late < 2) bar_raw();
  if (late == 3) {
    const u32x4 swo = mk_srd(A->wo, WO_BYTES);
    load_wo(swo, lb, lane, slot - 23, w - 8); load_wo(swo, lb, lane, slot - 23, w - 4);
    return tag;
  }
  if (late < 2) {
    if (LATEPOS) bar_raw(); else if (!OWNPOLL) __builtin_amdgcn_s_sleep(LS);
    if (slot != 24 && slot != 25) touch_run(A, sink, xcd, lane, U4 + (slot < 24 ? slot - 16 : slot - 18) + 12 * (w - 8), 225 + 81, 48);
    if (!LATEPOS) bar_raw();
    const u32x4 sq = mk_srd(A->q, ACT_BYTES);
    const int ch0 = ca * 64 + sub * 8;
    const u32 vq = late ? (lane >> 1) * 1152 + (lane & 1) * 16 : lane * 1152;
#pragma unroll 1
    for (int n = w - 8; n < (late ? 32 : 16); n += 4) {
      const int bl = late ? (n + 4 * sub) & 31 : (n + 2 * sub) & 15;
      dma16(sq, vq, (late ? bl * 32 : 1024 + bl * 64) * 1152 + ch0 * 2, lb + C_Q + bl * 1024);
    }
    if (OTA && w == 8) touch(sink, mk_srd(A->out, OUT_BYTES), lane < 2 ? lane * 1023 * 2304 : 0x80000000u, (late ? 0 : 1024) * 2304 + ch0 * 2);
    WAIT_VM(0);
    bar_raw();
  } else if (slot == 25 && xcd == 7 && w == 8) {
    poll<16>(A->ws + WS_CHECK + 7 * 128, tag);
    __builtin_amdgcn_s_sleep(8);
#pragma unroll 1
    for (int it = 0; it < SPIN_LIMIT; ++it) {
      bool ok = true;
#pragma unroll
      for (int n = 0; n < 16; ++n) ok &= ld_flag(A->ws + WS_CHECK + (n * 16 + (lane >> 2)) * 128 + (lane & 3) * 8) == tag;
      if (__all(ok)) break;
      __builtin_amdgcn_s_sleep(8);
    }
    if (lane == 0) st_flag(A->ws + WS_EPOCH, tag);
  }
  WAIT_VM(0);
  FIN();
  return 0;
}

#define STR2(x) #x
#define STR(x) STR2(x)
extern "C" __global__ __launch_bounds__(NT) void fused_kernel(Args) {
  __shared__ __attribute__((aligned(16))) char lds[LDS_BYTES];
  asm volatile(
      "s_load_dword s95, %0, 0x0\n\ts_load_dword s95, %0, 0x40\n\t"
      "v_readfirstlane_b32 s3, %2\n\t"
      "s_cmpk_lt_u32 s3, 0x200\n\ts_cbranch_scc1 1f\n\t"
      "s_cmpk_lt_u32 %1, 0x80\n\ts_cbranch_scc0 2f\n\t"
      "s_cmpk_lt_u32 s3, 0x240\n\ts_cbranch_scc0 r_load\n\ts_branch r_halo\n"
      "1:\n\ts_cmpk_lt_u32 %1, 0x80\n\ts_cbranch_scc1 r_bmain\n\t"
      "s_cmpk_lt_u32 %1, 0xf0\n\ts_cbranch_scc1 r_nmain\n\ts_branch r_bmain\n"
      "2:\n\ts_cmpk_lg_u32 s3, 0x280\n\ts_cbranch_scc1 r_naux\n\t"
      "s_lshr_b32 s3, %1, 3\n\ts_sub_u32 s3, s3, 25\n\ts_cmp_ge_u32 s3, " STR(CODE_UNITS) "\n\ts_cbranch_scc1 r_naux\n\t"
      "s_getpc_b64 s[4:5]\n\ts_and_b32 s4, s4, 0xffffff00\n\ts_lshl_b32 s3, s3, 6\n\t"
      "v_and_b32 v167, 63, %2\n\tv_add_lshl_u32 v167, v167, s3, 7\n\tv_min_u32 v167, r_end-fused_kernel, v167\n\tglobal_load_dword v167, v167, s[4:5]\n\ts_branch r_naux"
      :: "s"(__builtin_amdgcn_kernarg_segment_ptr()), "s"(blockIdx.x), "v"(threadIdx.x), "s"((u32)(size_t)(LAS char*)lds)
      : "s3", "s4", "s5", "s90", "s91", "s92", "s93", "s95", "v167", "scc", "vcc", "memory");
}
#define ROLE extern "C" __global__ __launch_bounds__(NT) void
#define ENTER()
#define IDS() ENTER(); const argp A = (argp)__builtin_amdgcn_kernarg_segment_ptr(); constexpr u32 lb = 0; \
  const int xcd = blockIdx.x & 7, slot = blockIdx.x >> 3, w = __builtin_amdgcn_readfirstlane(threadIdx.x >> 6), lane = threadIdx.x & 63; \
  const int i2 = 2 * xcd + (slot >= 9), cg = slot - (slot >= 9) * 9 - (slot >= 16) * 14;
template <int KIND>
DEV void aux_tail(DG argp A, Relay& R, u32 lb, int lane, int w, int i2, int cg, u64 tag) {
  u64 f = tag;
  if (KIND == 2 || !RFIRST) { WAIT_VM(KIND == 2 ? 9 : 10); relay_rd(R, 0); }
  if (KIND == 1 && CPOS == 0) f = ld_flag(A->ws + R.cp);
  SB();
  relay_wr(R, 0);
  if (KIND == 1 && CPOS == 1) f = ld_flag(A->ws + R.cp);
  WAIT_VM(KIND == 1);
  relay_rd(R, 1);
  relay_wr(R, 1);
  if (KIND == 1 ? !__all(f == tag) : KIND == 2 && w == 8) poll<KIND == 1 ? 2 : HG>(A->ws + R.cp, tag);
  STAMP(4);
  gemm2_aux(DA A->ws, lb, lane, w, i2, cg, R.vx, KIND == 2);
  FIN();
}
ROLE r_load(Args) { IDS(); Relay R; b_aux<true>(DA A, lb, xcd, slot, cg, i2, R, w, lane); aux_tail<0>(DA A, R, lb, lane, w, i2, cg, 0); }
ROLE r_naux(Args) { IDS(); const u64 tag = nb_aux(DA A, lb, xcd, slot, i2, w, lane); Relay R; relay_adr(R, lb, lane, w - 8, i2); aux_tail<2>(DA A, R, lb, lane, w, i2, cg, tag); }
ROLE r_halo(Args) { IDS(); Relay R; const u64 tag = b_aux<false>(DA A, lb, xcd, slot, cg, i2, R, w, lane); aux_tail<1>(DA A, R, lb, lane, w, i2, cg, tag); }
ROLE r_bmain(Args) { IDS(); if (__builtin_expect(slot < 16, 1)) b_main(DA A, lb, xcd, slot, w, lane); else bar_raw(); gemm2(DA A->ws, A->out, lb, lane, w, i2, cg); FIN(); }
ROLE r_nmain(Args) { IDS(); nb_main(DA A, lb, xcd, slot, w, lane); }
#define FITS(k) ".if (" #k ".private_seg_size | " #k ".num_agpr | " #k ".has_dyn_sized_stack | (" #k ".num_vgpr > 168) | (" #k ".numbered_sgpr > 88)) != 0\n\t.error \"role exceeds budget\"\n\t.endif\n\t"
ROLE r_end(Args) { asm volatile(FITS(r_load) FITS(r_naux) FITS(r_halo) FITS(r_bmain) FITS(r_nmain) ".if r_naux.num_vgpr > 160\n\t.error \"role exceeds budget\"\n\t.endif"); }

extern "C" void kernel_launch(void* const* d_in, const int* in_sizes, int n_in, void* d_out, int out_size, void* d_ws, size_t ws_size,
                              hipStream_t stream) {
  const Args a = {(const u16*)d_in[0], (const u16*)d_in[1], (const u16*)d_in[2], (const u16*)d_in[3], (const u16*)d_in[4], (const u16*)d_in[5],
                  (const u16*)d_in[6], (const u16*)d_in[7], (const u16*)d_in[8], (u16*)d_out, (char*)d_ws};
  fused_kernel<<<256, NT, 0, stream>>>(a);
}
```
